```python
import torch

def _softmax(x, axis=-1):
    return torch.softmax(x, dim=axis)
def _gelu(x):
    return torch.nn.functional.gelu(x, approximate="tanh")
def _silu(x):
    return torch.nn.functional.silu(x)
def _sigmoid(x):
    return torch.sigmoid(x)
def _layernorm(x, axis=-1, eps=1e-5):
    m = x.mean(dim=axis, keepdim=True)
    v = x.var(dim=axis, keepdim=True, unbiased=False)
    return (x - m) / torch.sqrt(v + eps)
def _scatter_add(tgt, idx, src, axis=0):
    return tgt.clone().index_add_(axis, idx, src)
def _segment_sum(src, seg, n):
    out = torch.zeros((n,) + src.shape[1:], dtype=src.dtype)
    return out.index_add_(0, seg, src)
def _pad(x, pw):
    flat = tuple(v for pair in reversed(pw) for v in pair)
    return torch.nn.functional.pad(x, flat)

kernel_name = 'sequential_preLN__ssm_conv_s4d__glu_gated_reglu__prompt'

def setup_inputs():

    _w = (lambda a: (lambda t: t.to(torch.bfloat16) if t.is_floating_point() else t)(torch.as_tensor(a)))
    def _A_setup():
        Bsz, L, D, N = 1, 2048, 512, 128
        return {
            "u": _w((lambda *s: torch.randn(*s, dtype=torch.bfloat16) * 0.05)(Bsz, L, D)),
            "log_A_real": _w((lambda *s: torch.randn(*s, dtype=torch.bfloat16) * 6.4)(D, N) * 0.5),
            "A_imag": _w((lambda *s: torch.randn(*s, dtype=torch.bfloat16) * 0.05)(D, N) * 3.14),
            "C_real": _w((lambda *s: torch.randn(*s, dtype=torch.bfloat16) * 0.05)(D, N) / N ** 0.5),
            "C_imag": _w((lambda *s: torch.randn(*s, dtype=torch.bfloat16) * 0.05)(D, N) / N ** 0.5),
            "log_dt": _w((lambda *s: torch.randn(*s, dtype=torch.bfloat16) * 0.05)(D) * 0.5 - 4.0),
            "D_skip": _w((lambda *s: torch.randn(*s, dtype=torch.bfloat16) * 0.05)(D)),
        }
    _A_in = _A_setup()

    def _B_setup():
        B, S, D, F = 1, 2048, 512, 1376
        return {
            "x": _w((lambda *s: torch.randn(*s, dtype=torch.bfloat16) * 0.05)(B, S, D)),
            "W_gate": _w((lambda *s: torch.randn(*s, dtype=torch.bfloat16) * 0.4)(D, F) * (2.0 / D) ** 0.5),
            "W_up": _w((lambda *s: torch.randn(*s, dtype=torch.bfloat16) * 0.4)(D, F) * (2.0 / D) ** 0.5),
            "W_down": _w((lambda *s: torch.randn(*s, dtype=torch.bfloat16) * 0.4)(F, D) * (1.0 / F) ** 0.5),
        }
    _B_in = _B_setup()

    return {
        "A_u": _A_in["u"],
        "A_log_A_real": _A_in["log_A_real"],
        "A_A_imag": _A_in["A_imag"],
        "A_C_real": _A_in["C_real"],
        "A_C_imag": _A_in["C_imag"],
        "A_log_dt": _A_in["log_dt"],
        "A_D_skip": _A_in["D_skip"],
        "B_x": _B_in["x"],
        "B_W_gate": _B_in["W_gate"],
        "B_W_up": _B_in["W_up"],
        "B_W_down": _B_in["W_down"],
    }

def reference(A_u, A_log_A_real, A_A_imag, A_C_real, A_C_imag, A_log_dt, A_D_skip, B_x, B_W_gate, B_W_up, B_W_down):
    def _A_ref(u, log_A_real, A_imag, C_real, C_imag, log_dt, D_skip):
        L = u.shape[1]
        dt = torch.exp(log_dt)
        A = -torch.exp(log_A_real) + 1j * A_imag
        C = (C_real + 1j * C_imag) * dt[:, None]
        pos = (lambda x, dt: x.to(dt))(torch.arange(L), log_dt.dtype)
        P = torch.exp((A * dt[:, None])[:, :, None] * pos[None, None, :])
        K = 2.0 * torch.einsum("dn,dnl->dl", C, P).real
        L2 = 2 * L
        u_t = (lambda x, *d: x.permute(*d))(u, 0, 2, 1)
        Kf = (lambda x, *a, **k: torch.fft.rfft(x.float(), *a, **k))(K, L2)
        Uf = (lambda x, *a, **k: torch.fft.rfft(x.float(), *a, **k))(u_t, L2)
        y = (lambda x, *a, **k: torch.fft.irfft(x, *a, **k).to(torch.bfloat16))(Uf * Kf[None, :, :], L2)[:, :, :L]
        y = (lambda x, *d: x.permute(*d))(y, 0, 2, 1)
        return y + u * D_skip[None, None, :]

    def _B_ref(x, W_gate, W_up, W_down):
        gate = torch.einsum("bsd,df->bsf", x, W_gate)
        up = torch.einsum("bsd,df->bsf", x, W_up)
        gate = gate * (lambda x, dt: x.to(dt))(gate > 0, gate.dtype)
        h = gate * up
        return torch.einsum("bsf,fd->bsd", h, W_down)

    x = A_u
    h = x + _A_ref(_layernorm(x, axis=-1), A_log_A_real, A_A_imag, A_C_real, A_C_imag, A_log_dt, A_D_skip)
    h2 = h + _B_ref(_layernorm(h, axis=-1), B_W_gate, B_W_up, B_W_down)
    return h2
```

```cpp
#include <hip/hip_runtime.h>
#include <hip/hip_bf16.h>
#include <stdint.h>

typedef unsigned short u16;
typedef __attribute__((ext_vector_type(8))) _Float16 f16x8;
typedef __attribute__((ext_vector_type(2))) _Float16 f16x2;
typedef __attribute__((ext_vector_type(8))) __bf16 bf16x8;
typedef __attribute__((ext_vector_type(4))) __bf16 bf16x4;
typedef __attribute__((ext_vector_type(2))) __bf16 bf16x2;
typedef __attribute__((ext_vector_type(4))) short s16x4;
typedef __attribute__((ext_vector_type(4))) float f32x4;
typedef __attribute__((ext_vector_type(2))) float f32x2;
typedef __attribute__((ext_vector_type(16))) float f32x16;
typedef __attribute__((ext_vector_type(4))) unsigned u32x4;
typedef __attribute__((ext_vector_type(2))) unsigned u32x2;
#define LAS __attribute__((address_space(3)))

namespace {

constexpr int L_ = 2048, D_ = 512, N_ = 128, F_ = 1376;
constexpr int KR_LEN = 2112;
constexpr int X0 = 2064;
constexpr int NFT = F_ / 16;
constexpr int NKS1 = D_ / 16;
constexpr int NLT = L_ / 32;

constexpr size_t WS_XT = 0;
constexpr size_t WS_HT = WS_XT + (size_t)D_ * L_ * 2;
constexpr size_t WS_KR = WS_HT + (size_t)D_ * L_ * 2;
constexpr size_t WS_ST1 = WS_KR + (size_t)D_ * KR_LEN * 2;
constexpr size_t WS_KSC = WS_ST1 + L_ * 4;
constexpr size_t WS_CS = WS_KSC + D_ * 4;
constexpr size_t WS_W1 = WS_CS + 4 * 2 * F_ * 4;
constexpr size_t WS_W2 = WS_W1 + (size_t)NFT * NKS1 * 1024;
constexpr size_t WS_HM = WS_W2 + (size_t)16 * NFT * 1024;
constexpr size_t WS_END = WS_HM + (size_t)NLT * NFT * 1024;

__device__ __forceinline__ float bf2f(u16 v) { return __uint_as_float((unsigned)v << 16); }
__device__ __forceinline__ float bflo(unsigned v) { return __uint_as_float(v << 16); }
__device__ __forceinline__ float bfhi(unsigned v) { return __uint_as_float(v & 0xffff0000u); }
__device__ __forceinline__ u16 f2bf(float f) {
  __bf16 b = (__bf16)f;
  return __builtin_bit_cast(u16, b);
}
__device__ __forceinline__ float rbf(float f) { return bf2f(f2bf(f)); }
__device__ __forceinline__ unsigned pack_bf2(float lo, float hi) {
  bf16x2 v;
  v[0] = (__bf16)lo;
  v[1] = (__bf16)hi;
  return __builtin_bit_cast(unsigned, v);
}
__device__ __forceinline__ unsigned pack_h2(float lo, float hi) {
  f16x2 v;
  v[0] = (_Float16)lo;
  v[1] = (_Float16)hi;
  return __builtin_bit_cast(unsigned, v);
}
__device__ __forceinline__ float wave_sum(float v) {
#pragma unroll
  for (int o = 32; o >= 1; o >>= 1) v += __shfl_xor(v, o, 64);
  return v;
}
__device__ __forceinline__ float wave_max(float v) {
#pragma unroll
  for (int o = 32; o >= 1; o >>= 1) v = fmaxf(v, __shfl_xor(v, o, 64));
  return v;
}

struct cplx {
  float r, i;
};
__device__ __forceinline__ cplx cmul(cplx a, cplx b) {
  cplx c;
  c.r = a.r * b.r - a.i * b.i;
  c.i = a.r * b.i + a.i * b.r;
  return c;
}

constexpr int K1_NB_KGEN = 512, K1_NB_LN = 128, K1_NB_W1 = 172, K1_NB_W2 = 86;
constexpr int K1_GRID = K1_NB_KGEN + K1_NB_LN + K1_NB_W1 + K1_NB_W2;
constexpr int VW_PITCH = 528;

__device__ __forceinline__ int tab_index(int t) {
  if (t < 256) return t;
  if (t < 512) return 256 + ((t - 256) >> 1);
  if (t < 1024) return 384 + ((t - 512) >> 2);
  return 512 + ((t - 1024) >> 3);
}

__device__ __forceinline__ void k1_kgen(char* smem, int d, const u16* __restrict__ lar, const u16* __restrict__ aim,
                                        const u16* __restrict__ cre, const u16* __restrict__ cim,
                                        const u16* __restrict__ logdt, u16* __restrict__ KrG,
                                        float* __restrict__ kscale) {
  const int tid = threadIdx.x, wave = tid >> 6, lane = tid & 63;
  char* Vs = smem;
  char* Ws = smem + 32 * VW_PITCH;
  float* Ft = (float*)(smem + 64 * VW_PITCH);
  float* misc = (float*)(smem + 64 * VW_PITCH + 2576);
  const int n = (wave & 1) * 64 + lane;
  const float dt = rbf(expf(bf2f(logdt[d])));
  const float ar = -rbf(expf(bf2f(lar[d * N_ + n]))) * dt;
  const float ai = bf2f(aim[d * N_ + n]) * dt;
  const float arl = ar * 1.4426950408889634f;
  const float C_HI = 0.15915494309189535f;
  const float C_LO = 6.4206383e-09f;
  const float ph = ai * C_HI;
  const float pl = fmaf(ai, C_HI, -ph) + ai * C_LO;
  auto zpow = [&](float t) -> cplx {
    float e = __builtin_amdgcn_exp2f(arl * t);
    float r = __builtin_amdgcn_fractf(ph * t) + pl * t;
    cplx z;
    z.r = e * __builtin_amdgcn_cosf(r);
    z.i = e * __builtin_amdgcn_sinf(r);
    return z;
  };
  if (wave >= 2) {
    cplx w[4];
    w[0].r = 1.f;
    w[0].i = 0.f;
    w[1] = zpow(8.f);
    w[2] = zpow(16.f);
    w[3] = cmul(w[1], w[2]);
    const cplx z32 = zpow(32.f);
#pragma unroll
    for (int g = 0; g < 8; ++g) {
#pragma unroll
      for (int e = 0; e < 4; ++e) {
        *(unsigned*)(Ws + (4 * g + e) * VW_PITCH + n * 4) = pack_h2(w[e].r, w[e].i);
        if (g < 7) w[e] = cmul(w[e], z32);
      }
    }
  } else {
    const float c0r = bf2f(cre[d * N_ + lane]), c0i = bf2f(cim[d * N_ + lane]);
    const float c1r = bf2f(cre[d * N_ + 64 + lane]), c1i = bf2f(cim[d * N_ + 64 + lane]);
    const float cmax = wave_max(fmaxf(fmaxf(fabsf(c0r), fabsf(c0i)), fmaxf(fabsf(c1r), fabsf(c1i))));
    const float csum = wave_sum(fabsf(c0r) + fabsf(c0i) + fabsf(c1r) + fabsf(c1i));
    const float inv = cmax > 0.f ? 256.f / cmax : 0.f;
    cplx c;
    c.r = (wave ? c1r : c0r) * inv;
    c.i = (wave ? c1i : c0i) * inv;
    if (tid == 0) {
      const float B = csum * inv;
      misc[0] = B > 0.f ? 16384.f / B : 0.f;
      kscale[d] = 2.f * dt * csum * (1.f / 16384.f);
    }
    auto put = [&](int row, cplx v) { *(unsigned*)(Vs + row * VW_PITCH + n * 4) = pack_h2(v.r, -v.i); };
    const cplx z1 = zpow(1.f), z2 = zpow(2.f), z4 = zpow(4.f);
    const cplx z3 = cmul(z1, z2), z5 = cmul(z4, z1), z6 = cmul(z4, z2);
    const cplx z7 = cmul(z4, z3);
    const cplx v1 = cmul(c, z1), v2 = cmul(c, z2), v3 = cmul(c, z3), v4 = cmul(c, z4);
    const cplx v5 = cmul(c, z5), v6 = cmul(c, z6), v7 = cmul(c, z7);
    put(0, c);
    put(1, v1);
    put(2, v2);
    put(3, v3);
    put(4, v4);
    put(5, v5);
    put(6, v6);
    put(7, v7);
    const cplx z256 = zpow(256.f), z512 = zpow(512.f), z1024 = zpow(1024.f), z2048 = zpow(2048.f);
    put(8, cmul(c, z256));
    put(9, cmul(v2, z256));
    put(10, cmul(v4, z256));
    put(11, cmul(v6, z256));
    const cplx z768 = cmul(z512, z256);
    put(12, cmul(c, z512));
    put(13, cmul(v4, z512));
    put(14, cmul(c, z768));
    put(15, cmul(v4, z768));
    const cplx v1024 = cmul(c, z1024);
    put(16, v1024);
    put(17, cmul(v1024, z256));
    put(18, cmul(v1024, z512));
    put(19, cmul(v1024, z768));
    put(20, cmul(c, z2048));
  }
  __syncthreads();
  {
    const int rt = wave >> 1, ct = wave & 1, idx = lane & 15, q = lane >> 4;
    f32x4 acc = {0.f, 0.f, 0.f, 0.f};
    const char* ap = Vs + (16 * rt + idx) * VW_PITCH + q * 16;
    const char* bp = Ws + (16 * ct + idx) * VW_PITCH + q * 16;
#pragma unroll
    for (int ks = 0; ks < 8; ++ks) {
      f16x8 a = *(const f16x8*)(ap + ks * 64);
      f16x8 b = *(const f16x8*)(bp + ks * 64);
      acc = __builtin_amdgcn_mfma_f32_16x16x32_f16(a, b, acc, 0, 0, 0);
    }
    const int j = 16 * ct + idx;
#pragma unroll
    for (int r = 0; r < 4; ++r) {
      const int row = 16 * rt + 4 * q + r;
      int ti = -1;
      if (row < 8) ti = row + 8 * j;
      else if (row < 12) ti = 256 + (row - 8) + 4 * j;
      else if (row < 16) ti = 384 + 64 * ((row - 12) >> 1) + ((row - 12) & 1) + 2 * j;
      else if (row < 20) ti = 512 + 32 * (row - 16) + j;
      else if (row == 20 && j == 0) ti = 640;
      if (ti >= 0) Ft[ti] = acc[r];
    }
  }
  __syncthreads();
  const float sK = misc[0];
  for (int c = tid; c < KR_LEN / 8; c += 256) {
    unsigned o[4];
#pragma unroll
    for (int e2 = 0; e2 < 4; ++e2) {
      float v[2];
#pragma unroll
      for (int e = 0; e < 2; ++e) {
        const int i = 8 * c + 2 * e2 + e;
        const int j = X0 + 8 - i;
        float val = 0.f;
        if (j >= 0 && j < L_) {
          const int t = (int)rbf((float)j);
          val = Ft[tab_index(t)] * sK;
        }
        v[e] = val;
      }
      o[e2] = pack_h2(v[0], v[1]);
    }
    *(u32x4*)(KrG + (size_t)d * KR_LEN + 8 * c) = (u32x4){o[0], o[1], o[2], o[3]};
  }
}

__device__ __forceinline__ void k1_ln(char* smem, int b, const u16* __restrict__ x, u16* __restrict__ xT,
                                      unsigned* __restrict__ st1) {
  const int tid = threadIdx.x, wave = tid >> 6, lane = tid & 63;
  constexpr int PITCH = 1024 + 16;
  const int l0 = b * 16;
  u32x4 v[4];
#pragma unroll
  for (int r = 0; r < 4; ++r) v[r] = *(const u32x4*)(x + (size_t)(l0 + wave * 4 + r) * D_ + lane * 8);
#pragma unroll
  for (int r = 0; r < 4; ++r) {
    float f[8];
#pragma unroll
    for (int k = 0; k < 4; ++k) {
      f[2 * k] = bflo(v[r][k]);
      f[2 * k + 1] = bfhi(v[r][k]);
    }
    float s = 0.f;
#pragma unroll
    for (int k = 0; k < 8; ++k) s += f[k];
    const float mean = wave_sum(s) * (1.f / D_);
    float q = 0.f;
#pragma unroll
    for (int k = 0; k < 8; ++k) q += (f[k] - mean) * (f[k] - mean);
    const float var = wave_sum(q) * (1.f / D_);
    if (lane == 0) {
      const float sd = rbf(sqrtf(rbf(rbf(var) + 1e-5f)));
      st1[l0 + wave * 4 + r] = (unsigned)f2bf(mean) | ((unsigned)f2bf(sd) << 16);
    }
    *(u32x4*)(smem + (wave * 4 + r) * PITCH + lane * 16) = v[r];
  }
  __syncthreads();
  unsigned w[16];
#pragma unroll
  for (int r = 0; r < 16; ++r) w[r] = *(const unsigned*)(smem + r * PITCH + tid * 4);
  u32x4 lo0, lo1, hi0, hi1;
#pragma unroll
  for (int k = 0; k < 4; ++k) {
    lo0[k] = (w[2 * k] & 0xffffu) | (w[2 * k + 1] << 16);
    lo1[k] = (w[8 + 2 * k] & 0xffffu) | (w[8 + 2 * k + 1] << 16);
    hi0[k] = (w[2 * k] >> 16) | (w[2 * k + 1] & 0xffff0000u);
    hi1[k] = (w[8 + 2 * k] >> 16) | (w[8 + 2 * k + 1] & 0xffff0000u);
  }
  u16* p0 = xT + (size_t)(2 * tid) * L_ + l0;
  u16* p1 = p0 + L_;
  *(u32x4*)(p0) = lo0;
  *(u32x4*)(p0 + 8) = lo1;
  *(u32x4*)(p1) = hi0;
  *(u32x4*)(p1 + 8) = hi1;
}

__device__ __forceinline__ void k1_w1(char* smem, int b, const u16* __restrict__ Wg, const u16* __restrict__ Wu,
                                      u32x4* __restrict__ W1t, float* __restrict__ cs) {
  const int tid = threadIdx.x, lane = tid & 63, wave = tid >> 6;
  const int fb = b % 43, db = b / 43;
  u16* M = (u16*)smem;
  constexpr int P = 40;
  float* red = (float*)(smem + 2 * 128 * P * 2);
#pragma unroll
  for (int i = 0; i < 4; ++i) {
    const int g = tid + 256 * i;
    const int mat = g >> 9, row = (g >> 2) & 127, c = g & 3;
    const u16* src = (mat ? Wu : Wg) + (size_t)(db * 128 + row) * F_ + fb * 32 + c * 8;
    *(u32x4*)(M + (mat * 128 + row) * P + c * 8) = *(const u32x4*)src;
  }
  __syncthreads();
  const int r = lane & 31, h = lane >> 5;
  const int ii = r >> 3, hh = (r >> 2) & 1, e = r & 3;
#pragma unroll
  for (int i = 0; i < 4; ++i) {
    const int fr = wave + 4 * i;
    const int tt = fr >> 3, kk = fr & 7;
    const u16* src = M + ((ii >> 1) * 128 + 16 * kk + 8 * h) * P + 16 * tt + 8 * hh + 4 * (ii & 1) + e;
    u32x4 o;
#pragma unroll
    for (int k = 0; k < 4; ++k) o[k] = (unsigned)src[(2 * k) * P] | ((unsigned)src[(2 * k + 1) * P] << 16);
    W1t[((size_t)(2 * fb + tt) * NKS1 + (8 * db + kk)) * 64 + lane] = o;
  }
  {
    const int col = tid & 63, qd = tid >> 6;
    const u16* src = M + ((col >> 5) * 128 + qd * 32) * P + (col & 31);
    float s = 0.f;
#pragma unroll 8
    for (int k = 0; k < 32; ++k) s += bf2f(src[k * P]);
    red[tid] = s;
  }
  __syncthreads();
  if (tid < 64) {
    const float s = red[tid] + red[tid + 64] + red[tid + 128] + red[tid + 192];
    cs[(db * 2 + (tid >> 5)) * F_ + fb * 32 + (tid & 31)] = s;
  }
}

__device__ __forceinline__ void k1_w2(char* smem, int b, const u16* __restrict__ Wd, u32x4* __restrict__ W2t) {
  const int tid = threadIdx.x, lane = tid & 63, wave = tid >> 6;
  u16* M = (u16*)smem;
  constexpr int P = 520;
#pragma unroll
  for (int i = 0; i < 4; ++i) {
    const int g = tid + 256 * i;
    const int row = g >> 6, c = g & 63;
    *(u32x4*)(M + row * P + c * 8) = *(const u32x4*)(Wd + (size_t)(b * 16 + row) * D_ + c * 8);
  }
  __syncthreads();
  const int r = lane & 31, h = lane >> 5;
#pragma unroll
  for (int i = 0; i < 4; ++i) {
    const int Dt = wave + 4 * i;
    const u16* src = M + (8 * h) * P + 32 * Dt + r;
    u32x4 o;
#pragma unroll
    for (int k = 0; k < 4; ++k) o[k] = (unsigned)src[(2 * k) * P] | ((unsigned)src[(2 * k + 1) * P] << 16);
    W2t[((size_t)Dt * NFT + b) * 64 + lane] = o;
  }
}

__global__ __launch_bounds__(256) void k1_prep(const u16* __restrict__ x, const u16* __restrict__ lar,
                                               const u16* __restrict__ aim, const u16* __restrict__ cre,
                                               const u16* __restrict__ cim, const u16* __restrict__ logdt,
                                               const u16* __restrict__ Wg, const u16* __restrict__ Wu,
                                               const u16* __restrict__ Wd, char* __restrict__ ws) {
  __shared__ __attribute__((aligned(16))) char smem[64 * VW_PITCH + 2576 + 64];
  const int b = blockIdx.x;
  if (b < K1_NB_KGEN) {
    k1_kgen(smem, b, lar, aim, cre, cim, logdt, (u16*)(ws + WS_KR), (float*)(ws + WS_KSC));
  } else if (b < K1_NB_KGEN + K1_NB_LN) {
    k1_ln(smem, b - K1_NB_KGEN, x, (u16*)(ws + WS_XT), (unsigned*)(ws + WS_ST1));
  } else if (b < K1_NB_KGEN + K1_NB_LN + K1_NB_W1) {
    k1_w1(smem, b - (K1_NB_KGEN + K1_NB_LN), Wg, Wu, (u32x4*)(ws + WS_W1), (float*)(ws + WS_CS));
  } else {
    k1_w2(smem, b - (K1_NB_KGEN + K1_NB_LN + K1_NB_W1), Wd, (u32x4*)(ws + WS_W2));
  }
}

constexpr int KC_STRIDE = 4288;
constexpr int KC_CH = 8 * KC_STRIDE;
constexpr int U_PITCH = 80;
constexpr int U_CH = 96 * U_PITCH;
constexpr int Y_PITCH = 144;
constexpr int Y_BUF = 64 * Y_PITCH;
constexpr int K2_LDS = 2 * KC_CH + 2 * U_CH;

__global__ __launch_bounds__(256) void k2_conv(const u16* __restrict__ dskip, char* __restrict__ ws) {
  extern __shared__ __attribute__((aligned(16))) char smem[];
  const int tid = threadIdx.x, wave = tid >> 6, lane = tid & 63;
  const int d0 = blockIdx.x * 2;
  const u16* xT = (const u16*)(ws + WS_XT);
  const u16* KrG = (const u16*)(ws + WS_KR);
  const unsigned* st1 = (const unsigned*)(ws + WS_ST1);
  const float* kscale = (const float*)(ws + WS_KSC);
  u16* hT = (u16*)(ws + WS_HT);
  char* KC = smem;
  char* US = smem + 2 * KC_CH;

  u32x4 xv[2];
  xv[0] = *(const u32x4*)(xT + (size_t)d0 * L_ + tid * 8);
  xv[1] = *(const u32x4*)(xT + (size_t)(d0 + 1) * L_ + tid * 8);
  const u32x4 sa = *(const u32x4*)(st1 + tid * 8), sb = *(const u32x4*)(st1 + tid * 8 + 4);
  {
    const int ch = wave >> 1, t = tid & 127;
    const u16* src = KrG + (size_t)(d0 + ch) * KR_LEN;
    char* dst = KC + ch * KC_CH;
#pragma unroll
    for (int it = 0; it < 3; ++it) {
      const int m = t + 128 * it;
      if (m < 262) {
        const u32x4 a = *(const u32x4*)(src + 8 * m), b = *(const u32x4*)(src + 8 * m + 8);
        const unsigned w[8] = {a[0], a[1], a[2], a[3], b[0], b[1], b[2], b[3]};
#pragma unroll
        for (int c = 0; c < 8; ++c) {
          const int s = 8 - c;
          u32x4 o;
          if ((s & 1) == 0) {
#pragma unroll
            for (int k = 0; k < 4; ++k) o[k] = w[s / 2 + k];
          } else {
#pragma unroll
            for (int k = 0; k < 4; ++k) o[k] = __builtin_amdgcn_alignbit(w[(s + 1) / 2 + k], w[(s - 1) / 2 + k], 16);
          }
          *(u32x4*)(dst + c * KC_STRIDE + m * 16) = o;
        }
      }
    }
  }
  {
    const int ch = tid >> 7, t = tid & 127;
    *(u32x4*)(US + ch * U_CH + (t >> 2) * U_PITCH + (t & 3) * 16) = (u32x4){0u, 0u, 0u, 0u};
  }
  float uf[2][8];
  {
    const unsigned st[8] = {sa[0], sa[1], sa[2], sa[3], sb[0], sb[1], sb[2], sb[3]};
#pragma unroll
    for (int ch = 0; ch < 2; ++ch) {
      unsigned o[4];
#pragma unroll
      for (int k = 0; k < 4; ++k) {
#pragma unroll
        for (int e = 0; e < 2; ++e) {
          const float xx = e ? bfhi(xv[ch][k]) : bflo(xv[ch][k]);
          const unsigned s = st[2 * k + e];
          const float dm = rbf(xx - bflo(s));
          uf[ch][2 * k + e] = rbf(dm / bfhi(s));
        }
        o[k] = pack_h2(uf[ch][2 * k], uf[ch][2 * k + 1]);
      }
      *(u32x4*)(US + ch * U_CH + (32 + (tid >> 2)) * U_PITCH + (tid & 3) * 16) = (u32x4){o[0], o[1], o[2], o[3]};
    }
  }
  __syncthreads();

  const int ch = wave >> 1, w2 = wave & 1;
  const int r = lane & 31, h = lane >> 5;
  f32x16 acc0 = {}, acc1 = {};
  {
    const char* abase = KC + ch * KC_CH + (r & 7) * KC_STRIDE + (X0 - 8 * (r >> 3) + 8 * h) * 2 - 64 * 16 * w2;
    const char* bbase = US + ch * U_CH + (r + 32) * U_PITCH + h * 16 - 16 * w2 * U_PITCH;
#pragma unroll
    for (int dd = 0; dd < 16; ++dd) {
#pragma unroll
      for (int hp = 0; hp < 2; ++hp) {
        const f16x8 a1 = *(const f16x8*)(abase - (2 * dd - hp) * 32);
        const f16x8 a2 = *(const f16x8*)(abase - (2 * dd - hp + 64) * 32);
        const f16x8 b1 = *(const f16x8*)(bbase - dd * U_PITCH + hp * 32);
        const f16x8 b2 = *(const f16x8*)(bbase + (32 - dd) * U_PITCH + hp * 32);
        acc0 = __builtin_amdgcn_mfma_f32_32x32x16_f16(a1, b1, acc0, 0, 0, 0);
        acc1 = __builtin_amdgcn_mfma_f32_32x32x16_f16(a1, b2, acc1, 0, 0, 0);
        acc1 = __builtin_amdgcn_mfma_f32_32x32x16_f16(a2, b1, acc1, 0, 0, 0);
      }
    }
  }
  __syncthreads();
  {
    char* yb = smem + (ch * 2 + w2) * Y_BUF;
#pragma unroll
    for (int i = 0; i < 4; ++i) {
      *(f32x4*)(yb + r * Y_PITCH + (8 * i + 4 * h) * 4) =
          (f32x4){acc0[4 * i], acc0[4 * i + 1], acc0[4 * i + 2], acc0[4 * i + 3]};
      *(f32x4*)(yb + (32 + r) * Y_PITCH + (8 * i + 4 * h) * 4) =
          (f32x4){acc1[4 * i], acc1[4 * i + 1], acc1[4 * i + 2], acc1[4 * i + 3]};
    }
  }
  __syncthreads();
#pragma unroll
  for (int c2 = 0; c2 < 2; ++c2) {
    const char* y0 = smem + (c2 * 2) * Y_BUF + (tid >> 2) * Y_PITCH + (tid & 3) * 32;
    const char* y1 = y0 + Y_BUF;
    const f32x4 p0 = *(const f32x4*)y0, p1 = *(const f32x4*)(y0 + 16);
    const f32x4 q0 = *(const f32x4*)y1, q1 = *(const f32x4*)(y1 + 16);
    const float ks = kscale[d0 + c2];
    const float dsk = bf2f(dskip[d0 + c2]);
    float hv[8];
#pragma unroll
    for (int k = 0; k < 8; ++k) {
      const float y = rbf(((k < 4 ? p0[k & 3] + q0[k & 3] : p1[k & 3] + q1[k & 3])) * ks);
      const float t = rbf(uf[c2][k] * dsk);
      const float a = rbf(y + t);
      const float xx = (k & 1) ? bfhi(xv[c2][k >> 1]) : bflo(xv[c2][k >> 1]);
      hv[k] = xx + a;
    }
    u32x4 o;
#pragma unroll
    for (int k = 0; k < 4; ++k) o[k] = pack_bf2(hv[2 * k], hv[2 * k + 1]);
    *(u32x4*)(hT + (size_t)(d0 + c2) * L_ + tid * 8) = o;
  }
}

constexpr int K3_RB = 22, K3_CG = 11;
constexpr int K3_APITCH = 192;
constexpr int K3_ACT = D_ * K3_APITCH;
constexpr int K3_LDS = K3_ACT + 8 * 96 * 8;

__device__ __forceinline__ void glds16(const void* gsrc, unsigned lds_dst) {
  unsigned keep;
  asm volatile(
      "s_mov_b32 %0, m0\n\ts_mov_b32 m0, %2\n\ts_nop 0\n\tglobal_load_lds_dwordx4 %1, off\n\ts_mov_b32 m0, %0"
      : "=&s"(keep)
      : "v"(gsrc), "s"(lds_dst)
      : "memory");
}
__device__ __forceinline__ u32x4 gload16_asm(const void* p) {
  u32x4 v;
  asm volatile("global_load_dwordx4 %0, %1, off" : "=v"(v) : "v"(p) : "memory");
  return v;
}
__device__ __forceinline__ bf16x8 tr_frag(const char* p) {
  s16x4 a = __builtin_amdgcn_ds_read_tr16_b64_v4i16((LAS s16x4*)p);
  s16x4 b = __builtin_amdgcn_ds_read_tr16_b64_v4i16((LAS s16x4*)(p + 4 * K3_APITCH));
  typedef __attribute__((ext_vector_type(8))) short s16x8;
  s16x8 v = {a[0], a[1], a[2], a[3], b[0], b[1], b[2], b[3]};
  return __builtin_bit_cast(bf16x8, v);
}

__global__ __launch_bounds__(512, 2) void k3_gemm1(char* __restrict__ ws) {
  extern __shared__ __attribute__((aligned(16))) char smem[];
  const int tid = threadIdx.x, lane = tid & 63;
  const int wave = __builtin_amdgcn_readfirstlane(tid >> 6);
  const int rb = blockIdx.x / K3_CG, cg = blockIdx.x % K3_CG;
  const int l0 = min(rb * 96, L_ - 96);
  const int Treal = cg * 8 + wave;
  const int T = min(Treal, NFT - 1);
  const u16* hT = (const u16*)(ws + WS_HT);
  const u32x4* W1t = (const u32x4*)(ws + WS_W1);
  const float* cs = (const float*)(ws + WS_CS);
  u32x4* hm = (u32x4*)(ws + WS_HM);
  const unsigned lds0 = (unsigned)(size_t)(LAS char*)smem;

  u32x4 wf[32];
  const u32x4* wp = W1t + (size_t)T * NKS1 * 64 + lane;
#pragma unroll
  for (int c = 0; c < 4; ++c) {
#pragma unroll
    for (int j = 0; j < 3; ++j) {
      const int i = 24 * c + 3 * wave + j;
      const int g = 64 * i + lane;
      const int dd = g / 12, cc = g - dd * 12;
      glds16(hT + (size_t)dd * L_ + l0 + 8 * cc, lds0 + i * 1024);
    }
#pragma unroll
    for (int k = 0; k < 8; ++k) wf[8 * c + k] = gload16_asm(wp + (8 * c + k) * 64);
  }
  const int r = lane & 31, h = lane >> 5;
  const int blk = (lane >> 4) & 1, q = (lane & 15) >> 2, p = lane & 3;
  const char* tb = smem + (8 * h + q) * K3_APITCH + (16 * blk + 4 * p) * 2;
  f32x16 acc[3] = {};
  float ssum[3] = {0.f, 0.f, 0.f}, ssq[3] = {0.f, 0.f, 0.f};
#pragma unroll
  for (int c = 0; c < 4; ++c) {
    if (c == 0) asm volatile("s_waitcnt vmcnt(33)" ::: "memory");
    if (c == 1) asm volatile("s_waitcnt vmcnt(22)" ::: "memory");
    if (c == 2) asm volatile("s_waitcnt vmcnt(11)" ::: "memory");
    if (c == 3) asm volatile("s_waitcnt vmcnt(0)" ::: "memory");
#pragma unroll
    for (int k = 0; k < 8; ++k) asm volatile("" : "+v"(wf[8 * c + k]));
    __builtin_amdgcn_s_barrier();
    asm volatile("" ::: "memory");
#pragma unroll
    for (int k = 0; k < 8; ++k) {
      const int ks = 8 * c + k;
      const bf16x8 a = __builtin_bit_cast(bf16x8, wf[ks]);
#pragma unroll
      for (int lt = 0; lt < 3; ++lt) {
        const bf16x8 b = tr_frag(tb + ks * 16 * K3_APITCH + lt * 64);
        acc[lt] = __builtin_amdgcn_mfma_f32_32x32x16_bf16(a, b, acc[lt], 0, 0, 0);
      }
    }
    {
      const char* sp = tb + (8 * c + wave) * 16 * K3_APITCH;
      const bf16x2 ones = {(__bf16)1.0f, (__bf16)1.0f};
#pragma unroll
      for (int lt = 0; lt < 3; ++lt) {
        const u32x4 b = __builtin_bit_cast(u32x4, tr_frag(sp + lt * 64));
#pragma unroll
        for (int k = 0; k < 4; ++k) {
          const unsigned bk = b[k];
          const bf16x2 v = __builtin_bit_cast(bf16x2, bk);
          ssum[lt] = __builtin_amdgcn_fdot2_f32_bf16(v, ones, ssum[lt], false);
          ssq[lt] = __builtin_amdgcn_fdot2_f32_bf16(v, v, ssq[lt], false);
        }
      }
    }
  }
  f32x2* sp = (f32x2*)(smem + K3_ACT);
#pragma unroll
  for (int lt = 0; lt < 3; ++lt) {
    ssum[lt] += __shfl_xor(ssum[lt], 32, 64);
    ssq[lt] += __shfl_xor(ssq[lt], 32, 64);
    if (h == 0) sp[wave * 96 + lt * 32 + r] = (f32x2){ssum[lt], ssq[lt]};
  }
  __syncthreads();
  float csg[8], csu[8];
#pragma unroll
  for (int k = 0; k < 8; ++k) {
    const int f = 16 * T + 8 * h + k;
    csg[k] = cs[0 * F_ + f] + cs[2 * F_ + f] + cs[4 * F_ + f] + cs[6 * F_ + f];
    csu[k] = cs[1 * F_ + f] + cs[3 * F_ + f] + cs[5 * F_ + f] + cs[7 * F_ + f];
  }
#pragma unroll
  for (int lt = 0; lt < 3; ++lt) {
    float s = 0.f, qq = 0.f;
#pragma unroll
    for (int w = 0; w < 8; ++w) {
      const f32x2 v = sp[w * 96 + lt * 32 + r];
      s += v[0];
      qq += v[1];
    }
    const float mean = s * (1.f / D_);
    const float var = fmaxf(qq * (1.f / D_) - mean * mean, 0.f);
    const float mb = rbf(mean);
    const float sd = rbf(sqrtf(rbf(rbf(var) + 1e-5f)));
    const float rs = 1.f / sd;
    float hv[8];
#pragma unroll
    for (int k = 0; k < 8; ++k) {
      const float g = rbf((acc[lt][k] - mb * csg[k]) * rs);
      const float u = rbf((acc[lt][8 + k] - mb * csu[k]) * rs);
      hv[k] = (g > 0.f ? g : 0.f) * u;
    }
    u32x4 o;
#pragma unroll
    for (int k = 0; k < 4; ++k) o[k] = pack_bf2(hv[2 * k], hv[2 * k + 1]);
    if (Treal < NFT) hm[((size_t)(l0 / 32 + lt) * NFT + T) * 64 + lane] = o;
  }
}

__global__ __launch_bounds__(256, 1) void k4_gemm2(const char* __restrict__ ws, u16* __restrict__ out) {
  __shared__ __attribute__((aligned(16))) char smem[4 * 3 * 4096];
  const int tid = threadIdx.x, lane = tid & 63;
  const int wave = __builtin_amdgcn_readfirstlane(tid >> 6);
  const int rb = blockIdx.x >> 3, cb = blockIdx.x & 7;
  const u16* hT = (const u16*)(ws + WS_HT);
  const u32x4* W2t = (const u32x4*)(ws + WS_W2);
  const u32x4* hm = (const u32x4*)(ws + WS_HM);
  const int ks0 = wave < 2 ? wave * 22 : 44 + (wave - 2) * 21;
  const int nks = wave < 2 ? 22 : 21;
  const int r = lane & 31, h = lane >> 5;
  const int dbase = cb * 64 + (wave >> 1) * 32, lpos = rb * 64 + (wave & 1) * 32 + r;
  u16 res[16];
#pragma unroll
  for (int k = 0; k < 16; ++k) res[k] = hT[(size_t)(dbase + 8 * (k >> 2) + 4 * h + (k & 3)) * L_ + lpos];

  const u32x4* a0p = W2t + ((size_t)(2 * cb) * NFT + ks0) * 64 + lane;
  const u32x4* a1p = a0p + (size_t)NFT * 64;
  const u32x4* b0p = hm + ((size_t)(2 * rb) * NFT + ks0) * 64 + lane;
  const u32x4* b1p = b0p + (size_t)NFT * 64;
  u32x4 A0[22], A1[22], B0[22], B1[22];
#pragma unroll
  for (int k = 0; k < 22; ++k) {
    const int kk = (k == 21) ? (nks - 1) : k;
    A0[k] = a0p[kk * 64];
    B0[k] = b0p[kk * 64];
    A1[k] = a1p[kk * 64];
    B1[k] = b1p[kk * 64];
  }
  __builtin_amdgcn_sched_barrier(0);
  f32x16 acc[2][2] = {};
#pragma unroll
  for (int k = 0; k < 22; ++k) {
    if (k < 21 || nks == 22) {
      const bf16x8 a0 = __builtin_bit_cast(bf16x8, A0[k]), a1 = __builtin_bit_cast(bf16x8, A1[k]);
      const bf16x8 b0 = __builtin_bit_cast(bf16x8, B0[k]), b1 = __builtin_bit_cast(bf16x8, B1[k]);
      acc[0][0] = __builtin_amdgcn_mfma_f32_32x32x16_bf16(a0, b0, acc[0][0], 0, 0, 0);
      acc[0][1] = __builtin_amdgcn_mfma_f32_32x32x16_bf16(a0, b1, acc[0][1], 0, 0, 0);
      acc[1][0] = __builtin_amdgcn_mfma_f32_32x32x16_bf16(a1, b0, acc[1][0], 0, 0, 0);
      acc[1][1] = __builtin_amdgcn_mfma_f32_32x32x16_bf16(a1, b1, acc[1][1], 0, 0, 0);
    }
  }
#pragma unroll
  for (int t = 0; t < 4; ++t) {
    if (t != wave) {
      const int slot = (wave - t - 1) & 3;
      char* dst = smem + (t * 3 + slot) * 4096 + lane * 16;
      const f32x16& a = acc[t >> 1][t & 1];
#pragma unroll
      for (int i = 0; i < 4; ++i) *(f32x4*)(dst + i * 1024) = (f32x4){a[4 * i], a[4 * i + 1], a[4 * i + 2], a[4 * i + 3]};
    }
  }
  __syncthreads();
  f32x16 fin;
  if (wave == 0) fin = acc[0][0];
  else if (wave == 1) fin = acc[0][1];
  else if (wave == 2) fin = acc[1][0];
  else fin = acc[1][1];
#pragma unroll
  for (int s = 0; s < 3; ++s) {
    const char* src = smem + (wave * 3 + s) * 4096 + lane * 16;
#pragma unroll
    for (int i = 0; i < 4; ++i) {
      const f32x4 v = *(const f32x4*)(src + i * 1024);
      fin[4 * i] += v[0];
      fin[4 * i + 1] += v[1];
      fin[4 * i + 2] += v[2];
      fin[4 * i + 3] += v[3];
    }
  }
  u16* op = out + (size_t)lpos * D_ + dbase + 4 * h;
#pragma unroll
  for (int i = 0; i < 4; ++i) {
    u32x2 o;
    o[0] = pack_bf2(bf2f(res[4 * i]) + rbf(fin[4 * i]), bf2f(res[4 * i + 1]) + rbf(fin[4 * i + 1]));
    o[1] = pack_bf2(bf2f(res[4 * i + 2]) + rbf(fin[4 * i + 2]), bf2f(res[4 * i + 3]) + rbf(fin[4 * i + 3]));
    *(u32x2*)(op + 8 * i) = o;
  }
}

}

extern "C" void kernel_launch(void* const* d_in, const int* in_sizes, int n_in, void* d_out, int out_size, void* d_ws,
                              size_t ws_size, hipStream_t stream) {
  const u16* x = (const u16*)d_in[0];
  const u16* lar = (const u16*)d_in[1];
  const u16* aim = (const u16*)d_in[2];
  const u16* cre = (const u16*)d_in[3];
  const u16* cim = (const u16*)d_in[4];
  const u16* logdt = (const u16*)d_in[5];
  const u16* dskip = (const u16*)d_in[6];
  const u16* Wg = (const u16*)d_in[8];
  const u16* Wu = (const u16*)d_in[9];
  const u16* Wd = (const u16*)d_in[10];
  char* ws = (char*)d_ws;
  (void)hipFuncSetAttribute((const void*)k2_conv, hipFuncAttributeMaxDynamicSharedMemorySize, K2_LDS);
  (void)hipFuncSetAttribute((const void*)k3_gemm1, hipFuncAttributeMaxDynamicSharedMemorySize, K3_LDS);
  k1_prep<<<K1_GRID, 256, 0, stream>>>(x, lar, aim, cre, cim, logdt, Wg, Wu, Wd, ws);
  k2_conv<<<D_ / 2, 256, K2_LDS, stream>>>(dskip, ws);
  k3_gemm1<<<K3_RB * K3_CG, 512, K3_LDS, stream>>>(ws);
  k4_gemm2<<<256, 256, 0, stream>>>(ws, (u16*)d_out);
}
```

```cpp
#include <hip/hip_runtime.h>
#include <hip/hip_bf16.h>
#include <stdint.h>

typedef unsigned short u16;
typedef __attribute__((ext_vector_type(8))) _Float16 f16x8;
typedef __attribute__((ext_vector_type(2))) _Float16 f16x2;
typedef __attribute__((ext_vector_type(8))) __bf16 bf16x8;
typedef __attribute__((ext_vector_type(2))) __bf16 bf16x2;
typedef __attribute__((ext_vector_type(4))) short s16x4;
typedef __attribute__((ext_vector_type(8))) short s16x8;
typedef __attribute__((ext_vector_type(4))) float f32x4;
typedef __attribute__((ext_vector_type(2))) float f32x2;
typedef __attribute__((ext_vector_type(16))) float f32x16;
typedef __attribute__((ext_vector_type(4))) unsigned u32x4;
typedef __attribute__((ext_vector_type(2))) unsigned u32x2;
#define LAS __attribute__((address_space(3)))
#define GAS __attribute__((address_space(1)))
typedef GAS unsigned gu32;
typedef GAS unsigned long long gu64;
#define RLX_AGENT __ATOMIC_RELAXED, __HIP_MEMORY_SCOPE_AGENT

#define STAMP_DECL
#define STAMP(kid, slot)
#define STAMP_FLUSH(kid)

namespace {

constexpr int L_ = 2048, D_ = 512, N_ = 128, F_ = 1376;
constexpr int KR_LEN = 2112;
constexpr int X0 = 2064;
constexpr int NFT = F_ / 16;
constexpr int NKS1 = D_ / 16;
constexpr int NLT = L_ / 32;

constexpr size_t WS_CTRL = 0;
constexpr size_t WS_FLAG = 4096;
constexpr size_t WS_XU = WS_FLAG + 256 * 256;
constexpr size_t WS_HT = WS_XU + (size_t)D_ * L_ * 8;
constexpr size_t WS_CSP = WS_HT + (size_t)22 * D_ * 96 * 2;
constexpr size_t WS_W1 = WS_CSP + (size_t)16 * NFT * 32 * 4;
constexpr size_t WS_W2 = WS_W1 + (size_t)NFT * NKS1 * 1024;
constexpr size_t WS_HM = WS_W2 + (size_t)16 * NFT * 1024;
constexpr size_t WS_END = WS_HM + (size_t)NLT * NFT * 1024;
constexpr unsigned SPIN_CAP = 1u << 18;

__device__ __forceinline__ float bf2f(u16 v) { return __uint_as_float((unsigned)v << 16); }
__device__ __forceinline__ float bflo(unsigned v) { return __uint_as_float(v << 16); }
__device__ __forceinline__ float bfhi(unsigned v) { return __uint_as_float(v & 0xffff0000u); }
__device__ __forceinline__ u16 f2bf(float f) {
  __bf16 b = (__bf16)f;
  return __builtin_bit_cast(u16, b);
}
__device__ __forceinline__ float rbf(float f) { return bf2f(f2bf(f)); }
__device__ __forceinline__ unsigned pack_bf2(float lo, float hi) {
  bf16x2 v;
  v[0] = (__bf16)lo;
  v[1] = (__bf16)hi;
  return __builtin_bit_cast(unsigned, v);
}
__device__ __forceinline__ unsigned pack_h2(float lo, float hi) {
  f16x2 v;
  v[0] = (_Float16)lo;
  v[1] = (_Float16)hi;
  return __builtin_bit_cast(unsigned, v);
}
template <int CTRL, int ROWMASK>
__device__ __forceinline__ float dpp_get(float v) {
  return __int_as_float(__builtin_amdgcn_update_dpp(0, __float_as_int(v), CTRL, ROWMASK, 0xf, true));
}
__device__ __forceinline__ float wave_sum(float v) {
  v += dpp_get<0x111, 0xf>(v);
  v += dpp_get<0x112, 0xf>(v);
  v += dpp_get<0x114, 0xf>(v);
  v += dpp_get<0x118, 0xf>(v);
  v += dpp_get<0x142, 0xa>(v);
  v += dpp_get<0x143, 0xc>(v);
  return __int_as_float(__builtin_amdgcn_readlane(__float_as_int(v), 63));
}
__device__ __forceinline__ float wave_max_nonneg(float v) {
  v = fmaxf(v, dpp_get<0x111, 0xf>(v));
  v = fmaxf(v, dpp_get<0x112, 0xf>(v));
  v = fmaxf(v, dpp_get<0x114, 0xf>(v));
  v = fmaxf(v, dpp_get<0x118, 0xf>(v));
  v = fmaxf(v, dpp_get<0x142, 0xa>(v));
  v = fmaxf(v, dpp_get<0x143, 0xc>(v));
  return __int_as_float(__builtin_amdgcn_readlane(__float_as_int(v), 63));
}
__device__ __forceinline__ unsigned next_epoch(unsigned c) { return c * 1664525u + 1013904223u; }

typedef f32x2 cplx;
__device__ __forceinline__ cplx cmul(cplx a, cplx b) {
  cplx t, c;
  asm("v_pk_mul_f32 %0, %1, %2 op_sel:[1,1] op_sel_hi:[1,0] neg_lo:[1,0]" : "=v"(t) : "v"(a), "v"(b));
  asm("v_pk_fma_f32 %0, %1, %2, %3 op_sel:[0,0,0] op_sel_hi:[0,1,1]" : "=v"(c) : "v"(a), "v"(b), "v"(t));
  return c;
}
__device__ __forceinline__ cplx csqr(cplx a) { return cmul(a, a); }
__device__ __forceinline__ float exp_acc(float x) {
  const float L2E = 1.4426950408889634f, L2E_LO = 1.9259630e-08f;
  const float hi = x * L2E;
  const float lo = fmaf(x, L2E, -hi) + x * L2E_LO;
  const float e = __builtin_amdgcn_exp2f(hi);
  return fmaf(e, lo * 0.6931471805599453f, e);
}
__device__ __forceinline__ s16x4 tr_read(const char* p) {
  return __builtin_amdgcn_ds_read_tr16_b64_v4i16((LAS s16x4*)p);
}
__device__ __forceinline__ u32x4 tr_pair(const char* p0, const char* p1) {
  const s16x4 a = tr_read(p0), b = tr_read(p1);
  const s16x8 v = {a[0], a[1], a[2], a[3], b[0], b[1], b[2], b[3]};
  return __builtin_bit_cast(u32x4, v);
}

struct KIdxTab {
  unsigned short v[KR_LEN];
  constexpr KIdxTab() : v() {
    for (int i = 0; i < KR_LEN; ++i) {
      const int j = X0 + 8 - i;
      int idx = 643;
      if (j >= 0 && j < L_) {
        if (j < 256) {
          idx = j;
        } else {
          int sh = 1;
          while ((j >> sh) >= 256) ++sh;
          int m = j >> sh;
          const int rem = j & ((1 << sh) - 1), half = 1 << (sh - 1);
          if (rem > half || (rem == half && (m & 1))) ++m;
          idx = 128 * sh + m;
        }
      }
      v[i] = (unsigned short)(2 * idx);
    }
  }
};
__device__ constexpr KIdxTab kidx_tab{};

constexpr int VW_PITCH = 528;
constexpr int VW_CH = 64 * VW_PITCH;
constexpr int KC_STRIDE = 4288;
constexpr int KC_CH = 8 * KC_STRIDE;
constexpr int U_PITCH = 80;
constexpr int U_CH = 96 * U_PITCH;
constexpr int Y_PITCH = 144;
constexpr int Y_BUF = 64 * Y_PITCH;
constexpr int FT_CH = 644 * 2 + 8;
constexpr int K12_KC = 0;
constexpr int K12_US = 2 * KC_CH;
constexpr int K12_FT = K12_US + 2 * U_CH;
constexpr int K12_MISC = K12_FT + 2 * FT_CH;
constexpr int K12_KL = K12_MISC + 32;
constexpr int K12_RT = K12_KL + 2 * KR_LEN * 2;
constexpr int RT_WAVE = 5248;
constexpr int K12_LDS = K12_RT + 4 * RT_WAVE;

__device__ __forceinline__ void retile_load(u32x4 (&v)[4], int u, int lane, const u16* __restrict__ Wg,
                                            const u16* __restrict__ Wu, const u16* __restrict__ Wd) {
  if (u < 688) {
    const int kb = u / 43, fb = u - kb * 43;
#pragma unroll
    for (int i = 0; i < 4; ++i) {
      const int c = lane + 64 * (i & 1);
      v[i] = *(const u32x4*)(((i >> 1) ? Wu : Wg) + (size_t)(32 * kb + (c >> 2)) * F_ + 32 * fb + 8 * (c & 3));
    }
  } else {
    const int u2 = u - 688, ks = u2 >> 2, dq = u2 & 3;
#pragma unroll
    for (int i = 0; i < 4; ++i) {
      const int c = lane + 64 * i;
      v[i] = *(const u32x4*)(Wd + (size_t)(16 * ks + (c >> 4)) * D_ + 128 * dq + 8 * (c & 15));
    }
  }
}
__device__ __forceinline__ void retile_store(char* rt, const u32x4 (&v)[4], int u, int lane, u32x4* __restrict__ W1t,
                                             u32x4* __restrict__ W2t, float* __restrict__ csp) {
  const int h = lane >> 5, g = lane >> 4, q = (lane & 15) >> 2, p = lane & 3;
  if (u < 688) {
    const int kb = u / 43, fb = u - kb * 43;
#pragma unroll
    for (int i = 0; i < 4; ++i) {
      const int c = lane + 64 * (i & 1);
      *(u32x4*)(rt + (i >> 1) * 2592 + (c >> 2) * 80 + (c & 3) * 16) = v[i];
    }
    const char* base = rt + (g & 1) * 2592 + (8 * h + q) * 80 + (8 * (p & 1) + 4 * (p >> 1)) * 2;
#pragma unroll
    for (int fr = 0; fr < 4; ++fr) {
      const int tt = fr >> 1, kk = fr & 1;
      const char* a = base + kk * 16 * 80 + tt * 32;
      W1t[((size_t)(2 * fb + tt) * NKS1 + (2 * kb + kk)) * 64 + lane] = tr_pair(a, a + 4 * 80);
    }
    {
      const int r = lane & 31, tt = lane >> 5, i = r >> 3;
      const char* src = rt + (i >> 1) * 2592 + (16 * tt + 8 * ((r >> 2) & 1) + 4 * (i & 1) + (r & 3)) * 2;
      float s0 = 0.f, s1 = 0.f;
#pragma unroll
      for (int k = 0; k < 32; k += 2) {
        s0 += bf2f(*(const u16*)(src + k * 80));
        s1 += bf2f(*(const u16*)(src + (k + 1) * 80));
      }
      csp[((size_t)(2 * fb + tt) * 32 + r) * 16 + kb] = s0 + s1;
    }
  } else {
    const int u2 = u - 688, ks = u2 >> 2, dq = u2 & 3;
#pragma unroll
    for (int i = 0; i < 4; ++i) {
      const int c = lane + 64 * i;
      *(u32x4*)(rt + (c >> 4) * 320 + (c & 15) * 16) = v[i];
    }
    const char* base = rt + (8 * h + q) * 320 + (16 * (g & 1) + 4 * p) * 2;
#pragma unroll
    for (int fr = 0; fr < 4; ++fr) {
      const char* a = base + fr * 64;
      W2t[((size_t)(4 * dq + fr) * NFT + ks) * 64 + lane] = tr_pair(a, a + 4 * 320);
    }
  }
}
__device__ __forceinline__ void gstore16_sc1(void* sbase, unsigned voff, u32x4 v) {
  asm volatile("global_store_dwordx4 %0, %1, %2 sc1\n\ts_nop 1" : : "v"(voff), "v"(v), "s"(sbase) : "memory");
}
template <int OFF>
__device__ __forceinline__ u32x4 gload16_sc1(const void* sbase, unsigned voff) {
  u32x4 v;
  asm volatile("global_load_dwordx4 %0, %1, %2 offset:%3 sc1" : "=v"(v) : "v"(voff), "s"(sbase), "i"(OFF) : "memory");
  return v;
}
#define WAITV(n) asm volatile("s_waitcnt vmcnt(" #n ")" ::: "memory")

__global__ __launch_bounds__(512, 2) void k12(const u16* __restrict__ x, const u16* __restrict__ lar,
                                              const u16* __restrict__ aim, const u16* __restrict__ cre,
                                              const u16* __restrict__ cim, const u16* __restrict__ logdt,
                                              const u16* __restrict__ dskip, const u16* __restrict__ Wg,
                                              const u16* __restrict__ Wu, const u16* __restrict__ Wd,
                                              char* __restrict__ ws) {
  extern __shared__ __attribute__((aligned(16))) char smem[];
  const int tid = threadIdx.x, lane = tid & 63;
  const int wave = __builtin_amdgcn_readfirstlane(tid >> 6);
  const int b = blockIdx.x;
  const int cp = (b & 7) * 32 + (b >> 3);
  const int d0 = 2 * cp;
  gu32* ctrl = (gu32*)(ws + WS_CTRL);
  u16* hT = (u16*)(ws + WS_HT);
  STAMP_DECL;
  STAMP(0, 0);

  const unsigned ctrl_old = __hip_atomic_load(ctrl, RLX_AGENT);
  const u32x4 rowv = *(const u32x4*)(x + (size_t)(8 * b + wave) * D_ + lane * 8);
  const int kc = wave >> 2, krole = (wave >> 1) & 1, n = (wave & 1) * 64 + lane;
  const int kd = d0 + kc;
  const u16 p_ldt = logdt[kd], p_lar = lar[kd * N_ + n], p_aim = aim[kd * N_ + n];
  u16 p_c0r = 0, p_c0i = 0, p_c1r = 0, p_c1i = 0;
  if (krole == 0) {
    p_c0r = cre[kd * N_ + lane];
    p_c0i = cim[kd * N_ + lane];
    p_c1r = cre[kd * N_ + 64 + lane];
    p_c1i = cim[kd * N_ + 64 + lane];
  }
  const unsigned dsk2 = *(const unsigned*)(dskip + d0);
  const u32x4 kix0 = *(const u32x4*)(kidx_tab.v + 8 * (tid & 255));
  const u32x4 kix1 = *(const u32x4*)(kidx_tab.v + 8 * (256 + (tid & 7)));
  const unsigned epoch = next_epoch(ctrl_old);

  {
    float f[8];
#pragma unroll
    for (int k = 0; k < 4; ++k) {
      f[2 * k] = bflo(rowv[k]);
      f[2 * k + 1] = bfhi(rowv[k]);
    }
    const float s = ((f[0] + f[1]) + (f[2] + f[3])) + ((f[4] + f[5]) + (f[6] + f[7]));
    const float mean = wave_sum(s) * (1.f / D_);
    float qv[8];
#pragma unroll
    for (int k = 0; k < 8; ++k) qv[k] = (f[k] - mean) * (f[k] - mean);
    const float var = wave_sum(((qv[0] + qv[1]) + (qv[2] + qv[3])) + ((qv[4] + qv[5]) + (qv[6] + qv[7]))) * (1.f / D_);
    const float mb = rbf(mean);
    const float rs = __builtin_amdgcn_rcpf(rbf(__builtin_amdgcn_sqrtf(rbf(rbf(var) + 1e-5f))));
    u32x4 o[2];
#pragma unroll
    for (int k = 0; k < 8; ++k) {
      const float u = rbf(rbf(f[k] - mb) * rs);
      const _Float16 uh = (_Float16)u;
      const unsigned xb = (k & 1) ? (rowv[k >> 1] >> 16) : (rowv[k >> 1] & 0xffffu);
      o[k >> 2][k & 3] = xb | ((unsigned)__builtin_bit_cast(u16, uh) << 16);
    }
    char* tt = smem + K12_RT;
    *(u32x4*)(tt + wave * 2064 + lane * 32) = o[0];
    *(u32x4*)(tt + wave * 2064 + lane * 32 + 16) = o[1];
    __syncthreads();
#pragma unroll
    for (int it = 0; it < 4; ++it) {
      const int chn = 64 * wave + 16 * it + (lane >> 2);
      const char* src = tt + (2 * (lane & 3)) * 2064 + chn * 4;
      const u32x4 v = {*(const unsigned*)src, epoch, *(const unsigned*)(src + 2064), epoch};
      gstore16_sc1(ws + WS_XU + (size_t)b * 64, chn * (L_ * 8) + (lane & 3) * 16, v);
    }
  }
  STAMP(0, 1);

  {
    char* Vs = smem + K12_KC + kc * VW_CH;
    char* Ws = Vs + 32 * VW_PITCH;
    const float dt = rbf(exp_acc(bf2f(p_ldt)));
    const float ar = -rbf(exp_acc(bf2f(p_lar))) * dt;
    const float ai = bf2f(p_aim) * dt;
    const float arl = ar * 1.4426950408889634f;
    const float C_HI = 0.15915493667125702f, C_LO = 6.4206383e-09f;
    const float ph = ai * C_HI;
    const float pl = fmaf(ai, C_HI, -ph) + ai * C_LO;
    auto zpow = [&](float t, float sg) -> cplx {
      const float e = __builtin_amdgcn_exp2f(arl * t);
      const float r = __builtin_amdgcn_fractf(ph * t) + pl * t;
      cplx z;
      z[0] = e * __builtin_amdgcn_cosf(r);
      z[1] = (sg * e) * __builtin_amdgcn_sinf(r);
      return z;
    };
    auto pk = [&](cplx v) { return pack_h2(v[0], v[1]); };
    if (krole == 1) {
      cplx w[4];
      w[0] = (cplx){1.f, 0.f};
      w[1] = zpow(8.f, 1.f);
      w[2] = csqr(w[1]);
      const cplx z32 = csqr(w[2]);
      w[3] = cmul(w[1], w[2]);
      char* wp = Ws + n * 4;
#pragma unroll
      for (int g = 0; g < 8; ++g) {
#pragma unroll
        for (int e = 0; e < 4; ++e) {
          *(unsigned*)(wp + (4 * g + e) * VW_PITCH) = pk(w[e]);
          if (g < 7) w[e] = cmul(w[e], z32);
        }
      }
    } else {
      const float c0r = bf2f(p_c0r), c0i = bf2f(p_c0i), c1r = bf2f(p_c1r), c1i = bf2f(p_c1i);
      const float cmax = wave_max_nonneg(fmaxf(fmaxf(fabsf(c0r), fabsf(c0i)), fmaxf(fabsf(c1r), fabsf(c1i))));
      const float csum = wave_sum((fabsf(c0r) + fabsf(c0i)) + (fabsf(c1r) + fabsf(c1i)));
      const float inv = cmax > 0.f ? 256.f / cmax : 0.f;
      cplx c;
      c[0] = ((wave & 1) ? c1r : c0r) * inv;
      c[1] = -((wave & 1) ? c1i : c0i) * inv;
      if ((wave & 1) == 0 && lane == 0) {
        const float B = csum * inv;
        float* misc = (float*)(smem + K12_MISC) + 2 * kc;
        misc[0] = B > 0.f ? 16384.f / B : 0.f;
        misc[1] = 2.f * dt * csum * (1.f / 16384.f);
      }
      char* vp = Vs + n * 4;
      auto put = [&](int row, cplx v) { *(unsigned*)(vp + row * VW_PITCH) = pk(v); };
      const cplx z1 = zpow(1.f, -1.f), z256 = zpow(256.f, -1.f);
      const cplx z2 = csqr(z1), z512 = csqr(z256);
      const cplx z4 = csqr(z2), z1024 = csqr(z512);
      const cplx z3 = cmul(z1, z2), z768 = cmul(z512, z256), z2048 = csqr(z1024);
      const cplx v1 = cmul(c, z1), v2 = cmul(c, z2), v4 = cmul(c, z4), v3 = cmul(c, z3);
      const cplx v1024 = cmul(c, z1024);
      put(0, c);
      put(1, v1);
      put(2, v2);
      put(3, v3);
      put(4, v4);
      put(5, cmul(v4, z1));
      const cplx v6 = cmul(v4, z2);
      put(6, v6);
      put(7, cmul(v4, z3));
      put(8, cmul(c, z256));
      put(9, cmul(v2, z256));
      put(10, cmul(v4, z256));
      put(11, cmul(v6, z256));
      put(12, cmul(c, z512));
      put(13, cmul(v4, z512));
      put(14, cmul(c, z768));
      put(15, cmul(v4, z768));
      put(16, v1024);
      put(17, cmul(v1024, z256));
      put(18, cmul(v1024, z512));
      put(19, cmul(v1024, z768));
      put(20, cmul(c, z2048));
    }
  }
  STAMP(0, 2);
  __syncthreads();
  u32x4 rtv[4];
  const int gw = b * 4 + (wave - 4);
  if (wave >= 4) retile_load(rtv, gw, lane, Wg, Wu, Wd);
  {
    const char* Vs = smem + K12_KC + kc * VW_CH;
    const char* Ws = Vs + 32 * VW_PITCH;
    _Float16* Ft = (_Float16*)(smem + K12_FT + kc * FT_CH);
    const int rt = (wave >> 1) & 1, ct = wave & 1, idx = lane & 15, q = lane >> 4;
    f32x4 acc = {0.f, 0.f, 0.f, 0.f};
    const char* ap = Vs + (16 * rt + idx) * VW_PITCH + q * 16;
    const char* bp = Ws + (16 * ct + idx) * VW_PITCH + q * 16;
    f16x8 a[8], bb[8];
#pragma unroll
    for (int ks = 0; ks < 8; ++ks) {
      a[ks] = *(const f16x8*)(ap + ks * 64);
      bb[ks] = *(const f16x8*)(bp + ks * 64);
    }
    const float sK = ((const float*)(smem + K12_MISC))[2 * kc];
    const int j = 16 * ct + idx;
    int t0, dr, dr2;
    if (rt == 0) {
      t0 = q < 2 ? 4 * q + 8 * j : (q == 2 ? 256 + 4 * j : 384 + 2 * j);
      dr = 1;
      dr2 = q == 3 ? 64 : 2;
    } else {
      t0 = q == 0 ? 512 + j : ((q == 1 && j == 0) ? 640 : 642);
      dr = q == 0 ? 32 : ((q == 1 && j == 0) ? 2 : 0);
      dr2 = q == 0 ? 64 : 0;
    }
#pragma unroll
    for (int ks = 0; ks < 8; ++ks) acc = __builtin_amdgcn_mfma_f32_16x16x32_f16(a[ks], bb[ks], acc, 0, 0, 0);
    Ft[t0] = (_Float16)(acc[0] * sK);
    {
      const int t1 = min(t0 + dr, 642);
      Ft[t1] = (_Float16)(acc[1] * sK);
      const int t2 = (rt == 1 && q != 0) ? 642 : t0 + dr2;
      Ft[t2] = (_Float16)(acc[2] * sK);
      const int t3 = (rt == 1 && q != 0) ? 642 : t0 + dr2 + dr;
      Ft[t3] = (_Float16)(acc[3] * sK);
    }
    if (lane == 0 && (wave & 3) == 0) Ft[643] = (_Float16)0.f;
  }
  STAMP(0, 3);
  __syncthreads();
  {
    const int c = tid >> 8, t = tid & 255;
    const char* Ft = smem + K12_FT + c * FT_CH;
    char* Kl = smem + K12_KL + c * KR_LEN * 2;
    auto expand = [&](u32x4 ix, int ch) {
      u32x4 o;
#pragma unroll
      for (int k = 0; k < 4; ++k) {
        const unsigned lo = *(const u16*)(Ft + (ix[k] & 0xffffu)), hi = *(const u16*)(Ft + (ix[k] >> 16));
        o[k] = lo | (hi << 16);
      }
      *(u32x4*)(Kl + ch * 16) = o;
    };
    expand(kix0, t);
    if (t < 8) expand(kix1, 256 + t);
  }
  STAMP(0, 4);
  __syncthreads();
  {
    const int c = tid >> 8, t = tid & 255;
    const char* Kl = smem + K12_KL + c * KR_LEN * 2;
    char* dst = smem + K12_KC + c * KC_CH;
    auto copies = [&](int m) {
      const u32x4 a = *(const u32x4*)(Kl + 16 * m), bq = *(const u32x4*)(Kl + 16 * m + 16);
      const unsigned w[8] = {a[0], a[1], a[2], a[3], bq[0], bq[1], bq[2], bq[3]};
#pragma unroll
      for (int cc = 0; cc < 8; ++cc) {
        const int s = 8 - cc;
        u32x4 o;
        if ((s & 1) == 0) {
#pragma unroll
          for (int k = 0; k < 4; ++k) o[k] = w[s / 2 + k];
        } else {
#pragma unroll
          for (int k = 0; k < 4; ++k) o[k] = __builtin_amdgcn_alignbit(w[(s + 1) / 2 + k], w[(s - 1) / 2 + k], 16);
        }
        *(u32x4*)(dst + cc * KC_STRIDE + m * 16) = o;
      }
    };
    copies(t);
    if (t < 6) copies(256 + t);
    if (tid >= 256)
      *(u32x4*)(smem + K12_US + ((tid >> 7) & 1) * U_CH + ((tid & 127) >> 2) * U_PITCH + (tid & 3) * 16) = (u32x4){0u, 0u, 0u, 0u};
  }
  STAMP(0, 5);
  unsigned xu[2][4];
  {
    const char* g0 = ws + WS_XU + (size_t)d0 * L_ * 8;
    const char* g1 = g0 + (size_t)L_ * 8;
    for (unsigned spins = 0;; ++spins) {
      u32x4 a0 = gload16_sc1<0>(g0, tid * 32), a1 = gload16_sc1<16>(g0, tid * 32);
      u32x4 b0 = gload16_sc1<0>(g1, tid * 32), b1 = gload16_sc1<16>(g1, tid * 32);
      WAITV(0);
      asm volatile("" : "+v"(a0), "+v"(a1), "+v"(b0), "+v"(b1));
      const bool ok = a0[1] == epoch && a0[3] == epoch && a1[1] == epoch && a1[3] == epoch && b0[1] == epoch &&
                      b0[3] == epoch && b1[1] == epoch && b1[3] == epoch;
      xu[0][0] = a0[0]; xu[0][1] = a0[2]; xu[0][2] = a1[0]; xu[0][3] = a1[2];
      xu[1][0] = b0[0]; xu[1][1] = b0[2]; xu[1][2] = b1[0]; xu[1][3] = b1[2];
      if (__all(ok) || spins > SPIN_CAP) break;
      __builtin_amdgcn_s_sleep(4);
    }
  }
#pragma unroll
  for (int c = 0; c < 2; ++c)
    *(u32x2*)(smem + K12_US + c * U_CH + (32 + (tid >> 3)) * U_PITCH + (tid & 7) * 8) =
        (u32x2){__builtin_amdgcn_perm(xu[c][1], xu[c][0], 0x07060302u), __builtin_amdgcn_perm(xu[c][3], xu[c][2], 0x07060302u)};
  STAMP(0, 6);
  __syncthreads();
  if (b == 0 && tid == 0) __hip_atomic_store(ctrl, epoch, RLX_AGENT);
  STAMP(0, 7);

  const int r = lane & 31, h = lane >> 5;
  f32x16 acc0 = {}, acc1 = {};
  if (wave < 4) {
    const int ch = wave >> 1, w2 = wave & 1;
    const char* abase = smem + K12_KC + ch * KC_CH + (r & 7) * KC_STRIDE + (X0 - 8 * (r >> 3) + 8 * h) * 2 - 64 * 16 * w2;
    const char* bbase = smem + K12_US + ch * U_CH + (r + 32) * U_PITCH + h * 16 - 16 * w2 * U_PITCH;
    f16x8 A1[3], A2[3], B1[3], B2[3];
#define CONV_LOAD(it_)                                                           \
  do {                                                                           \
    const int dd_ = (it_) >> 1, hp_ = (it_) & 1, s_ = (it_) % 3;                 \
    A1[s_] = *(const f16x8*)(abase - (2 * dd_ - hp_) * 32);                      \
    B1[s_] = *(const f16x8*)(bbase - dd_ * U_PITCH + hp_ * 32);                  \
    B2[s_] = *(const f16x8*)(bbase + (32 - dd_) * U_PITCH + hp_ * 32);           \
    A2[s_] = *(const f16x8*)(abase - (2 * dd_ - hp_ + 64) * 32);                 \
  } while (0)
    CONV_LOAD(0);
    CONV_LOAD(1);
#pragma unroll
    for (int it = 0; it < 32; ++it) {
      if (it + 2 < 32) CONV_LOAD(it + 2);
      __builtin_amdgcn_sched_barrier(0);
      const int s = it % 3;
      acc0 = __builtin_amdgcn_mfma_f32_32x32x16_f16(A1[s], B1[s], acc0, 0, 0, 0);
      acc1 = __builtin_amdgcn_mfma_f32_32x32x16_f16(A1[s], B2[s], acc1, 0, 0, 0);
      acc1 = __builtin_amdgcn_mfma_f32_32x32x16_f16(A2[s], B1[s], acc1, 0, 0, 0);
      __builtin_amdgcn_sched_barrier(0);
    }
  } else {
    char* rt = smem + K12_RT + (wave - 4) * RT_WAVE;
    retile_store(rt, rtv, gw, lane, (u32x4*)(ws + WS_W1), (u32x4*)(ws + WS_W2), (float*)(ws + WS_CSP));
    if (gw < 8) {
      retile_load(rtv, 1024 + gw, lane, Wg, Wu, Wd);
      retile_store(rt, rtv, 1024 + gw, lane, (u32x4*)(ws + WS_W1), (u32x4*)(ws + WS_W2), (float*)(ws + WS_CSP));
    }
  }
  STAMP(0, 8);
  __syncthreads();
  if (wave < 4) {
    char* yb = smem + wave * Y_BUF;
#pragma unroll
    for (int i = 0; i < 4; ++i) {
      *(f32x4*)(yb + r * Y_PITCH + (8 * i + 4 * h) * 4) =
          (f32x4){acc0[4 * i], acc0[4 * i + 1], acc0[4 * i + 2], acc0[4 * i + 3]};
      *(f32x4*)(yb + (32 + r) * Y_PITCH + (8 * i + 4 * h) * 4) =
          (f32x4){acc1[4 * i], acc1[4 * i + 1], acc1[4 * i + 2], acc1[4 * i + 3]};
    }
  }
  __syncthreads();
  STAMP(0, 9);
  const int lp = 4 * tid, rbl = lp / 96;
#pragma unroll
  for (int c = 0; c < 2; ++c) {
    const char* y0 = smem + (c * 2) * Y_BUF + (tid >> 3) * Y_PITCH + (tid & 7) * 16;
    const f32x4 p0 = *(const f32x4*)y0, p1 = *(const f32x4*)(y0 + Y_BUF);
    const float ks = ((const float*)(smem + K12_MISC))[2 * c + 1];
    const float dsk = c ? bfhi(dsk2) : bflo(dsk2);
    float hv[4];
#pragma unroll
    for (int k = 0; k < 4; ++k) {
      const unsigned w = xu[c][k];
      const float y = rbf((p0[k] + p1[k]) * ks);
      const float uu = (float)__builtin_bit_cast(_Float16, (u16)(w >> 16));
      const float t = rbf(uu * dsk);
      const float a = rbf(y + t);
      hv[k] = bflo(w) + a;
    }
    const u32x2 o = {pack_bf2(hv[0], hv[1]), pack_bf2(hv[2], hv[3])};
    if (lp < 2016) *(u32x2*)(hT + ((size_t)rbl * D_ + d0 + c) * 96 + (lp - 96 * rbl)) = o;
    if (lp >= 1952) *(u32x2*)(hT + ((size_t)21 * D_ + d0 + c) * 96 + (lp - 1952)) = o;
  }
  STAMP(0, 15);
  STAMP_FLUSH(0);
}

constexpr int K3_RB = 22, K3_CG = 11, K3_TILES = K3_RB * K3_CG;
constexpr int K3_APITCH = 192;
constexpr int K3_ACT = D_ * K3_APITCH;
constexpr int K3_ST = K3_ACT;
constexpr int K3_CSX = K3_ST + 8 * 96 * 8;
constexpr int K34_RED = 8 * 7 * 2048;
constexpr int K34_LDS = K34_RED > (K3_CSX + 1024) ? K34_RED : (K3_CSX + 1024);

__device__ __forceinline__ void glds16(const void* sbase, unsigned voff, unsigned lds_dst) {
  unsigned keep;
  asm volatile(
      "s_mov_b32 %0, m0\n\ts_mov_b32 m0, %3\n\ts_nop 0\n\tglobal_load_lds_dwordx4 %1, %2\n\ts_mov_b32 m0, %0"
      : "=&s"(keep)
      : "v"(voff), "s"(sbase), "s"(lds_dst));
}
template <int OFF, int SC1>
__device__ __forceinline__ u32x4 gld(const void* sbase, unsigned voff) {
  u32x4 v;
  if (SC1)
    asm volatile("global_load_dwordx4 %0, %1, %2 offset:%3 sc1" : "=v"(v) : "v"(voff), "s"(sbase), "i"(OFF));
  else
    asm volatile("global_load_dwordx4 %0, %1, %2 offset:%3" : "=v"(v) : "v"(voff), "s"(sbase), "i"(OFF));
  return v;
}
__device__ __forceinline__ unsigned gld_u32(const void* sbase, unsigned voff) {
  unsigned v;
  asm volatile("global_load_dword %0, %1, %2" : "=v"(v) : "v"(voff), "s"(sbase));
  return v;
}
__device__ __forceinline__ unsigned gld_u16(const void* sbase, unsigned voff) {
  unsigned v;
  asm volatile("global_load_ushort %0, %1, %2" : "=v"(v) : "v"(voff), "s"(sbase));
  return v;
}
#define FRAG(base, k, sc1) gld<((k) & 7) * 1024 - 4096, sc1>(base, vo[(k) >> 3])
#define WAITR(n, reg) asm volatile("s_waitcnt vmcnt(" #n ")" : "+v"(reg))
#define BARRIER()                    \
  do {                               \
    asm volatile("" ::: "memory");   \
    __builtin_amdgcn_s_barrier();    \
    asm volatile("" ::: "memory");   \
  } while (0)

__global__ __launch_bounds__(512, 2) void k34(char* __restrict__ ws, u16* __restrict__ out) {
  extern __shared__ __attribute__((aligned(16))) char smem[];
  const int tid = threadIdx.x, lane = tid & 63;
  const int wave = __builtin_amdgcn_readfirstlane(tid >> 6);
  const u16* hT = (const u16*)(ws + WS_HT);
  gu32* flags = (gu32*)(ws + WS_FLAG);
  const unsigned lds0 = (unsigned)(size_t)(LAS char*)smem;
  const int r = lane & 31, h = lane >> 5;
  const unsigned lane16 = lane * 16;
  unsigned vo[4];
#pragma unroll
  for (int j = 0; j < 4; ++j) vo[j] = lane16 + 4096 + j * 8192;
  const int xcd = blockIdx.x & 7, slot = blockIdx.x >> 3;
  const int rb2 = 4 * xcd + (slot >> 3), cb = slot & 7;
  const int ks0 = wave < 6 ? wave * 11 : 66 + (wave - 6) * 10;
  const bool k11 = wave < 6;
  const int own = wave >> 1, half = wave & 1;
  const int dbase = cb * 64 + (own >> 1) * 32 + 16 * half, lpos = rb2 * 64 + (own & 1) * 32 + r;
  u32x4 fr[32];
  unsigned res[8];
  STAMP_DECL;
  STAMP(1, 0);
#define LOAD_RES()                                                                                         \
  do {                                                                                                     \
    const int rbl = min(lpos / 96, 21), lo = lpos - (rbl == 21 ? 1952 : 96 * rbl);                         \
    const unsigned ro = ((rbl * D_ + dbase + 4 * h) * 96 + lo) * 2;                                        \
    _Pragma("unroll") for (int k = 0; k < 8; ++k) res[k] =                                                 \
        gld_u16((const char*)hT + (8 * (k >> 2) + (k & 3)) * 192, ro);                                     \
  } while (0)
  const unsigned epoch = __hip_atomic_load((gu32*)(ws + WS_CTRL), RLX_AGENT);

  const int p1 = 30 * xcd + min(xcd, 2) + slot;
  const bool has1 = slot < (xcd < 2 ? 31 : 30);
  const char* a0p = ws + WS_W2 + ((size_t)(2 * cb) * NFT + ks0) * 1024;
  const char* a1p = a0p + (size_t)NFT * 1024;
  if (has1) {
    int rb, cg;
    if (p1 < 132) {
      rb = p1 / 6;
      cg = p1 - rb * 6;
    } else {
      const int p2 = p1 - 132;
      rb = p2 / 5;
      cg = 6 + p2 - rb * 5;
    }
    const int l0 = min(rb * 96, L_ - 96);
    const int Treal = cg * 8 + wave;
    const int T = min(Treal, NFT - 1);
    const char* wp = ws + WS_W1 + (size_t)T * NKS1 * 1024;
    const char* hp = (const char*)hT + (size_t)rb * (D_ * 192) + 3 * wave * 1024;
    const unsigned ldsw = lds0 + 3 * wave * 1024;
    u32x4 csp[2];
#define LOAD_CSP()                                                                      \
  do {                                                                                  \
    const char* cp_ = ws + WS_CSP + (size_t)T * 2048;                                   \
    const unsigned co = r * 64 + h * 32;                                                \
    csp[0] = gld<0, 0>(cp_, co);                                                        \
    csp[1] = gld<16, 0>(cp_, co);                                                       \
  } while (0)
    const int blk = (lane >> 4) & 1, q = (lane & 15) >> 2, p = lane & 3;
    const char* tb = smem + (8 * h + q) * K3_APITCH + (16 * blk + 4 * p) * 2;
    f32x16 acc[3] = {};
    f32x2 ssum[3] = {}, ssq[3] = {};
    u32x4 bq[4];
    f32x2* sp = (f32x2*)(smem + K3_ST);
    float* csx = (float*)(smem + K3_CSX) + wave * 32;
    float cs[16], mb[3], rs2[3];
#define BREAD(buf, ks, lt)                                                            \
  do {                                                                                \
    const char* bp_ = tb + (ks) * 16 * K3_APITCH + (lt) * 64;                         \
    bq[buf] = tr_pair(bp_, bp_ + 4 * K3_APITCH);                                      \
  } while (0)
#define MMA1(buf, ks, lt)                                                                                  \
  do {                                                                                                     \
    acc[lt] = __builtin_amdgcn_mfma_f32_32x32x16_bf16(__builtin_bit_cast(bf16x8, fr[ks]),                  \
                                                      __builtin_bit_cast(bf16x8, bq[buf]), acc[lt], 0, 0, 0); \
    asm volatile("" : "+v"(acc[lt]));              \
  } while (0)
#define EPI(lt)                                                                       \
  do {                                                                                \
    float hv[8];                                                                      \
    _Pragma("unroll") for (int k = 0; k < 8; ++k) {                                   \
      const float g = fmaf(-mb[lt], cs[k], acc[lt][k]);                               \
      const float u = fmaf(-mb[lt], cs[8 + k], acc[lt][8 + k]);                       \
      hv[k] = fmaxf(g, 0.f) * rs2[lt] * u;                                            \
    }                                                                                 \
    u32x4 o;                                                                          \
    _Pragma("unroll") for (int k = 0; k < 4; ++k) o[k] = pack_bf2(hv[2 * k], hv[2 * k + 1]); \
    gstore16_sc1(hmp + (size_t)(lt) * NFT * 1024, lane16, o);                         \
  } while (0)
    char* hmp = Treal < NFT ? ws + WS_HM + ((size_t)(l0 / 32) * NFT + T) * 1024 : ws + WS_END + 65536;
    auto stats = [&](int c, int lt) {
      const char* s0 = tb + (8 * c + wave) * 16 * K3_APITCH + lt * 64;
      const u32x4 b4 = tr_pair(s0, s0 + 4 * K3_APITCH);
#pragma unroll
      for (int k = 0; k < 4; ++k) {
        const f32x2 v = {bflo(b4[k]), bfhi(b4[k])};
        ssum[lt] += v;
        ssq[lt] = __builtin_elementwise_fma(v, v, ssq[lt]);
      }
    };
    auto publish_partials = [&]() {
      const f32x4 c4 = __builtin_bit_cast(f32x4, csp[0]) + __builtin_bit_cast(f32x4, csp[1]);
      const float csl = (c4[0] + c4[1]) + (c4[2] + c4[3]);
      float v[7] = {ssum[0][0] + ssum[0][1], ssq[0][0] + ssq[0][1], ssum[1][0] + ssum[1][1], ssq[1][0] + ssq[1][1],
                    ssum[2][0] + ssum[2][1], ssq[2][0] + ssq[2][1], csl};
#pragma unroll
      for (int i = 0; i < 7; ++i) {
        const auto sw = __builtin_amdgcn_permlane32_swap(__float_as_uint(v[i]), __float_as_uint(v[i]), false, false);
        v[i] = __uint_as_float(sw[0]) + __uint_as_float(sw[1]);
      }
      sp[wave * 96 + h * 32 + r] = h ? (f32x2){v[2], v[3]} : (f32x2){v[0], v[1]};
      if (h == 0) sp[wave * 96 + 64 + r] = (f32x2){v[4], v[5]};
      else csx[r] = v[6];
    };
    float m2[2], q2[2];
    auto finish_round = [&](int rd) {
      const int li = rd ? 64 + r : lane;
      float s = 0.f, qq = 0.f;
#pragma unroll
      for (int w = 0; w < 8; ++w) {
        const f32x2 v = sp[w * 96 + li];
        s += v[0];
        qq += v[1];
      }
      const float mean = s * (1.f / D_);
      const float var = fmaxf(qq * (1.f / D_) - mean * mean, 0.f);
      const float sd = rbf(__builtin_amdgcn_sqrtf(rbf(rbf(var) + 1e-5f)));
      const float rs = __builtin_amdgcn_rcpf(sd);
      m2[rd] = rbf(mean);
      q2[rd] = rs * rs;
    };
    auto finish_rest = [&]() {
#pragma unroll
      for (int i = 0; i < 4; ++i) {
        const f32x4 v = *(const f32x4*)(csx + 8 * i + 4 * h);
        cs[4 * i] = v[0];
        cs[4 * i + 1] = v[1];
        cs[4 * i + 2] = v[2];
        cs[4 * i + 3] = v[3];
      }
      const auto sm = __builtin_amdgcn_permlane32_swap(__float_as_uint(m2[0]), __float_as_uint(m2[0]), false, false);
      const auto sq = __builtin_amdgcn_permlane32_swap(__float_as_uint(q2[0]), __float_as_uint(q2[0]), false, false);
      mb[0] = __uint_as_float(sm[0]);
      mb[1] = __uint_as_float(sm[1]);
      rs2[0] = __uint_as_float(sq[0]);
      rs2[1] = __uint_as_float(sq[1]);
      mb[2] = m2[1];
      rs2[2] = q2[1];
    };

    glds16(hp + 0, lane16, ldsw + 0);
    glds16(hp + 1024, lane16, ldsw + 1024);
    glds16(hp + 2048, lane16, ldsw + 2048);
    fr[0] = FRAG(wp, 0, 0);
    fr[1] = FRAG(wp, 1, 0);
    fr[2] = FRAG(wp, 2, 0);
    fr[3] = FRAG(wp, 3, 0);
    fr[4] = FRAG(wp, 4, 0);
    fr[5] = FRAG(wp, 5, 0);
    fr[6] = FRAG(wp, 6, 0);
    fr[7] = FRAG(wp, 7, 0);
    glds16(hp + 24576, lane16, ldsw + 24576);
    glds16(hp + 25600, lane16, ldsw + 25600);
    glds16(hp + 26624, lane16, ldsw + 26624);
    fr[8] = FRAG(wp, 8, 0);
    fr[9] = FRAG(wp, 9, 0);
    STAMP(1, 1);
    WAITR(13, vo[0]);
    BARRIER();
    STAMP(1, 2);
    BREAD(0, 0, 0);
    BREAD(1, 0, 1);
    BREAD(2, 1, 0);
    BREAD(3, 1, 1);
    WAITR(12, fr[0]);
    MMA1(0, 0, 0);
    MMA1(1, 0, 1);
    fr[10] = FRAG(wp, 10, 0);
    fr[11] = FRAG(wp, 11, 0);
    __builtin_amdgcn_sched_barrier(0);
    BREAD(0, 2, 0);
    BREAD(1, 2, 1);
    WAITR(13, fr[1]);
    MMA1(2, 1, 0);
    MMA1(3, 1, 1);
    fr[12] = FRAG(wp, 12, 0);
    stats(0, 0);
    __builtin_amdgcn_sched_barrier(0);
    BREAD(2, 3, 0);
    BREAD(3, 3, 1);
    WAITR(13, fr[2]);
    MMA1(0, 2, 0);
    MMA1(1, 2, 1);
    fr[13] = FRAG(wp, 13, 0);
    fr[14] = FRAG(wp, 14, 0);
    __builtin_amdgcn_sched_barrier(0);
    BREAD(0, 4, 0);
    BREAD(1, 4, 1);
    WAITR(14, fr[3]);
    MMA1(2, 3, 0);
    MMA1(3, 3, 1);
    fr[15] = FRAG(wp, 15, 0);
    stats(0, 1);
    __builtin_amdgcn_sched_barrier(0);
    BREAD(2, 5, 0);
    BREAD(3, 5, 1);
    WAITR(14, fr[4]);
    MMA1(0, 4, 0);
    MMA1(1, 4, 1);
    glds16(hp + 49152, lane16, ldsw + 49152);
    glds16(hp + 50176, lane16, ldsw + 50176);
    __builtin_amdgcn_sched_barrier(0);
    BREAD(0, 6, 0);
    BREAD(1, 6, 1);
    WAITR(15, fr[5]);
    MMA1(2, 5, 0);
    MMA1(3, 5, 1);
    glds16(hp + 51200, lane16, ldsw + 51200);
    stats(0, 2);
    __builtin_amdgcn_sched_barrier(0);
    BREAD(2, 7, 0);
    BREAD(3, 7, 1);
    WAITR(15, fr[6]);
    MMA1(0, 6, 0);
    MMA1(1, 6, 1);
    fr[16] = FRAG(wp, 16, 0);
    fr[17] = FRAG(wp, 17, 0);
    __builtin_amdgcn_sched_barrier(0);
    WAITR(13, vo[0]);
    BARRIER();
    BREAD(0, 8, 0);
    BREAD(1, 8, 1);
    WAITR(16, fr[7]);
    MMA1(2, 7, 0);
    MMA1(3, 7, 1);
    fr[18] = FRAG(wp, 18, 0);
    STAMP(1, 3);
    __builtin_amdgcn_sched_barrier(0);
    BREAD(2, 9, 0);
    BREAD(3, 9, 1);
    WAITR(13, fr[8]);
    MMA1(0, 8, 0);
    MMA1(1, 8, 1);
    fr[19] = FRAG(wp, 19, 0);
    fr[20] = FRAG(wp, 20, 0);
    __builtin_amdgcn_sched_barrier(0);
    BREAD(0, 10, 0);
    BREAD(1, 10, 1);
    WAITR(14, fr[9]);
    MMA1(2, 9, 0);
    MMA1(3, 9, 1);
    fr[21] = FRAG(wp, 21, 0);
    stats(1, 0);
    __builtin_amdgcn_sched_barrier(0);
    BREAD(2, 11, 0);
    BREAD(3, 11, 1);
    WAITR(14, fr[10]);
    MMA1(0, 10, 0);
    MMA1(1, 10, 1);
    fr[22] = FRAG(wp, 22, 0);
    fr[23] = FRAG(wp, 23, 0);
    __builtin_amdgcn_sched_barrier(0);
    BREAD(0, 12, 0);
    BREAD(1, 12, 1);
    WAITR(15, fr[11]);
    MMA1(2, 11, 0);
    MMA1(3, 11, 1);
    glds16(hp + 73728, lane16, ldsw + 73728);
    stats(1, 1);
    __builtin_amdgcn_sched_barrier(0);
    BREAD(2, 13, 0);
    BREAD(3, 13, 1);
    WAITR(15, fr[12]);
    MMA1(0, 12, 0);
    MMA1(1, 12, 1);
    glds16(hp + 74752, lane16, ldsw + 74752);
    glds16(hp + 75776, lane16, ldsw + 75776);
    __builtin_amdgcn_sched_barrier(0);
    BREAD(0, 14, 0);
    BREAD(1, 14, 1);
    WAITR(16, fr[13]);
    MMA1(2, 13, 0);
    MMA1(3, 13, 1);
    fr[24] = FRAG(wp, 24, 0);
    stats(1, 2);
    __builtin_amdgcn_sched_barrier(0);
    BREAD(2, 15, 0);
    BREAD(3, 15, 1);
    WAITR(16, fr[14]);
    MMA1(0, 14, 0);
    MMA1(1, 14, 1);
    fr[25] = FRAG(wp, 25, 0);
    fr[26] = FRAG(wp, 26, 0);
    __builtin_amdgcn_sched_barrier(0);
    WAITR(14, vo[0]);
    BARRIER();
    BREAD(0, 16, 0);
    BREAD(1, 16, 1);
    WAITR(17, fr[15]);
    MMA1(2, 15, 0);
    MMA1(3, 15, 1);
    fr[27] = FRAG(wp, 27, 0);
    STAMP(1, 4);
    __builtin_amdgcn_sched_barrier(0);
    BREAD(2, 17, 0);
    BREAD(3, 17, 1);
    WAITR(14, fr[16]);
    MMA1(0, 16, 0);
    MMA1(1, 16, 1);
    fr[28] = FRAG(wp, 28, 0);
    fr[29] = FRAG(wp, 29, 0);
    __builtin_amdgcn_sched_barrier(0);
    BREAD(0, 18, 0);
    BREAD(1, 18, 1);
    WAITR(15, fr[17]);
    MMA1(2, 17, 0);
    MMA1(3, 17, 1);
    fr[30] = FRAG(wp, 30, 0);
    stats(2, 0);
    __builtin_amdgcn_sched_barrier(0);
    BREAD(2, 19, 0);
    BREAD(3, 19, 1);
    WAITR(15, fr[18]);
    MMA1(0, 18, 0);
    MMA1(1, 18, 1);
    fr[31] = FRAG(wp, 31, 0);
    LOAD_CSP();
    __builtin_amdgcn_sched_barrier(0);
    BREAD(0, 20, 0);
    BREAD(1, 20, 1);
    WAITR(17, fr[19]);
    MMA1(2, 19, 0);
    MMA1(3, 19, 1);
    stats(2, 1);
    __builtin_amdgcn_sched_barrier(0);
    BREAD(2, 21, 0);
    BREAD(3, 21, 1);
    WAITR(16, fr[20]);
    MMA1(0, 20, 0);
    MMA1(1, 20, 1);
    __builtin_amdgcn_sched_barrier(0);
    BREAD(0, 22, 0);
    BREAD(1, 22, 1);
    WAITR(15, fr[21]);
    MMA1(2, 21, 0);
    MMA1(3, 21, 1);
    stats(2, 2);
    __builtin_amdgcn_sched_barrier(0);
    BREAD(2, 23, 0);
    BREAD(3, 23, 1);
    WAITR(14, fr[22]);
    MMA1(0, 22, 0);
    MMA1(1, 22, 1);
    __builtin_amdgcn_sched_barrier(0);
    WAITR(10, vo[0]);
    BARRIER();
    BREAD(0, 24, 0);
    BREAD(1, 24, 1);
    WAITR(13, fr[23]);
    MMA1(2, 23, 0);
    MMA1(3, 23, 1);
    __builtin_amdgcn_sched_barrier(0);
    BREAD(2, 25, 0);
    BREAD(3, 25, 1);
    WAITR(9, fr[24]);
    MMA1(0, 24, 0);
    MMA1(1, 24, 1);
    stats(3, 0);
    __builtin_amdgcn_sched_barrier(0);
    BREAD(0, 26, 0);
    BREAD(1, 26, 1);
    WAITR(8, fr[25]);
    MMA1(2, 25, 0);
    MMA1(3, 25, 1);
    stats(3, 1);
    __builtin_amdgcn_sched_barrier(0);
    BREAD(2, 27, 0);
    BREAD(3, 27, 1);
    WAITR(7, fr[26]);
    MMA1(0, 26, 0);
    MMA1(1, 26, 1);
    stats(3, 2);
    __builtin_amdgcn_sched_barrier(0);
    BREAD(0, 28, 0);
    BREAD(1, 28, 1);
    WAITR(6, fr[27]);
    MMA1(2, 27, 0);
    MMA1(3, 27, 1);
    __builtin_amdgcn_sched_barrier(0);
    BREAD(2, 29, 0);
    BREAD(3, 29, 1);
    WAITR(5, fr[28]);
    MMA1(0, 28, 0);
    MMA1(1, 28, 1);
    WAITR(0, csp[0]);
    asm volatile("" : "+v"(csp[1]));
    publish_partials();
    __builtin_amdgcn_sched_barrier(0);
    BARRIER();
    BREAD(0, 30, 0);
    BREAD(1, 30, 1);
    WAITR(4, fr[29]);
    MMA1(2, 29, 0);
    MMA1(3, 29, 1);
    __builtin_amdgcn_sched_barrier(0);
    BREAD(2, 31, 0);
    BREAD(3, 31, 1);
    WAITR(3, fr[30]);
    MMA1(0, 30, 0);
    MMA1(1, 30, 1);
    finish_round(0);
    __builtin_amdgcn_sched_barrier(0);
    BREAD(0, 0, 2);
    WAITR(2, fr[31]);
    MMA1(2, 31, 0);
    MMA1(3, 31, 1);
    finish_round(1);
    __builtin_amdgcn_sched_barrier(0);
    STAMP(1, 5);
    BREAD(1, 1, 2);
    MMA1(0, 0, 2);
    finish_rest();
    __builtin_amdgcn_sched_barrier(0);
    BREAD(0, 2, 2);
    MMA1(1, 1, 2);
    __builtin_amdgcn_sched_barrier(0);
    BREAD(1, 3, 2);
    MMA1(0, 2, 2);
    EPI(0);
    __builtin_amdgcn_sched_barrier(0);
    BREAD(0, 4, 2);
    MMA1(1, 3, 2);
    __builtin_amdgcn_sched_barrier(0);
    BREAD(1, 5, 2);
    MMA1(0, 4, 2);
    __builtin_amdgcn_sched_barrier(0);
    BREAD(0, 6, 2);
    MMA1(1, 5, 2);
    __builtin_amdgcn_sched_barrier(0);
    BREAD(1, 7, 2);
    MMA1(0, 6, 2);
    EPI(1);
    __builtin_amdgcn_sched_barrier(0);
    BREAD(0, 8, 2);
    MMA1(1, 7, 2);
    __builtin_amdgcn_sched_barrier(0);
    BREAD(1, 9, 2);
    MMA1(0, 8, 2);
    fr[0] = FRAG(a0p, 0, 0);
    __builtin_amdgcn_sched_barrier(0);
    BREAD(0, 10, 2);
    MMA1(1, 9, 2);
    fr[1] = FRAG(a1p, 0, 0);
    __builtin_amdgcn_sched_barrier(0);
    BREAD(1, 11, 2);
    MMA1(0, 10, 2);
    fr[2] = FRAG(a0p, 1, 0);
    __builtin_amdgcn_sched_barrier(0);
    BREAD(0, 12, 2);
    MMA1(1, 11, 2);
    fr[3] = FRAG(a1p, 1, 0);
    __builtin_amdgcn_sched_barrier(0);
    BREAD(1, 13, 2);
    MMA1(0, 12, 2);
    fr[4] = FRAG(a0p, 2, 0);
    __builtin_amdgcn_sched_barrier(0);
    BREAD(0, 14, 2);
    MMA1(1, 13, 2);
    fr[5] = FRAG(a1p, 2, 0);
    __builtin_amdgcn_sched_barrier(0);
    BREAD(1, 15, 2);
    MMA1(0, 14, 2);
    fr[6] = FRAG(a0p, 3, 0);
    __builtin_amdgcn_sched_barrier(0);
    BREAD(0, 16, 2);
    MMA1(1, 15, 2);
    fr[7] = FRAG(a1p, 3, 0);
    __builtin_amdgcn_sched_barrier(0);
    BREAD(1, 17, 2);
    MMA1(0, 16, 2);
    fr[8] = FRAG(a0p, 4, 0);
    __builtin_amdgcn_sched_barrier(0);
    BREAD(0, 18, 2);
    MMA1(1, 17, 2);
    fr[9] = FRAG(a1p, 4, 0);
    __builtin_amdgcn_sched_barrier(0);
    BREAD(1, 19, 2);
    MMA1(0, 18, 2);
    fr[10] = FRAG(a0p, 5, 0);
    __builtin_amdgcn_sched_barrier(0);
    BREAD(0, 20, 2);
    MMA1(1, 19, 2);
    fr[11] = FRAG(a1p, 5, 0);
    __builtin_amdgcn_sched_barrier(0);
    BREAD(1, 21, 2);
    MMA1(0, 20, 2);
    fr[12] = FRAG(a0p, 6, 0);
    __builtin_amdgcn_sched_barrier(0);
    BREAD(0, 22, 2);
    MMA1(1, 21, 2);
    fr[13] = FRAG(a1p, 6, 0);
    __builtin_amdgcn_sched_barrier(0);
    BREAD(1, 23, 2);
    MMA1(0, 22, 2);
    fr[14] = FRAG(a0p, 7, 0);
    __builtin_amdgcn_sched_barrier(0);
    BREAD(0, 24, 2);
    MMA1(1, 23, 2);
    fr[15] = FRAG(a1p, 7, 0);
    __builtin_amdgcn_sched_barrier(0);
    BREAD(1, 25, 2);
    MMA1(0, 24, 2);
    fr[16] = FRAG(a0p, 8, 0);
    __builtin_amdgcn_sched_barrier(0);
    BREAD(0, 26, 2);
    MMA1(1, 25, 2);
    fr[17] = FRAG(a1p, 8, 0);
    __builtin_amdgcn_sched_barrier(0);
    BREAD(1, 27, 2);
    MMA1(0, 26, 2);
    fr[18] = FRAG(a0p, 9, 0);
    __builtin_amdgcn_sched_barrier(0);
    BREAD(0, 28, 2);
    MMA1(1, 27, 2);
    fr[19] = FRAG(a1p, 9, 0);
    __builtin_amdgcn_sched_barrier(0);
    BREAD(1, 29, 2);
    MMA1(0, 28, 2);
    fr[20] = FRAG(a0p, 10, 0);
    __builtin_amdgcn_sched_barrier(0);
    BREAD(0, 30, 2);
    MMA1(1, 29, 2);
    fr[21] = FRAG(a1p, 10, 0);
    __builtin_amdgcn_sched_barrier(0);
    BREAD(1, 31, 2);
    MMA1(0, 30, 2);
    __builtin_amdgcn_sched_barrier(0);
    MMA1(1, 31, 2);
    __builtin_amdgcn_sched_barrier(0);
    STAMP(1, 6);
    EPI(2);
    STAMP(1, 7);
    WAITV(0);
    __syncthreads();
    if (tid == 0) __hip_atomic_store(flags + (rb * K3_CG + cg) * 64, epoch, RLX_AGENT);

    LOAD_RES();
  } else {
    fr[0] = FRAG(a0p, 0, 0);
    fr[1] = FRAG(a1p, 0, 0);
    fr[2] = FRAG(a0p, 1, 0);
    fr[3] = FRAG(a1p, 1, 0);
    fr[4] = FRAG(a0p, 2, 0);
    fr[5] = FRAG(a1p, 2, 0);
    fr[6] = FRAG(a0p, 3, 0);
    fr[7] = FRAG(a1p, 3, 0);
    fr[8] = FRAG(a0p, 4, 0);
    fr[9] = FRAG(a1p, 4, 0);
    fr[10] = FRAG(a0p, 5, 0);
    fr[11] = FRAG(a1p, 5, 0);
    fr[12] = FRAG(a0p, 6, 0);
    fr[13] = FRAG(a1p, 6, 0);
    fr[14] = FRAG(a0p, 7, 0);
    fr[15] = FRAG(a1p, 7, 0);
    fr[16] = FRAG(a0p, 8, 0);
    fr[17] = FRAG(a1p, 8, 0);
    fr[18] = FRAG(a0p, 9, 0);
    fr[19] = FRAG(a1p, 9, 0);
    fr[20] = FRAG(a0p, 10, 0);
    fr[21] = FRAG(a1p, 10, 0);
    LOAD_RES();
  }
  STAMP(1, 8);

  {
    const char* b0p = ws + WS_HM + ((size_t)(2 * rb2) * NFT + ks0) * 1024;
    const char* b1p = b0p + (size_t)NFT * 1024;
    u32x4 B0[11], B1[11];
    if (wave == 0) {
      const int lA = 64 * rb2, lB = lA + 63;
      const int rA = min(lA / 96, K3_RB - 1), rB = min(lB / 96, K3_RB - 1);
      const int nfl = (rB - rA + 1) * K3_CG;
      const int fi = rA * K3_CG + min(lane, nfl - 1);
      for (unsigned spins = 0;; ++spins) {
        const unsigned v = __hip_atomic_load(flags + fi * 64, RLX_AGENT);
        if (__all(v == epoch) || spins > SPIN_CAP) break;
        __builtin_amdgcn_s_sleep(1);
      }
    }
    __syncthreads();
    STAMP(1, 9);
#define MMA2(k)                                                                                                   \
  do {                                                                                                            \
    const bf16x8 a0 = __builtin_bit_cast(bf16x8, fr[2 * (k)]), a1 = __builtin_bit_cast(bf16x8, fr[2 * (k) + 1]);  \
    const bf16x8 b0 = __builtin_bit_cast(bf16x8, B0[k]), b1 = __builtin_bit_cast(bf16x8, B1[k]);                  \
    acc[0][0] = __builtin_amdgcn_mfma_f32_32x32x16_bf16(a0, b0, acc[0][0], 0, 0, 0);                              \
    acc[0][1] = __builtin_amdgcn_mfma_f32_32x32x16_bf16(a0, b1, acc[0][1], 0, 0, 0);                              \
    acc[1][0] = __builtin_amdgcn_mfma_f32_32x32x16_bf16(a1, b0, acc[1][0], 0, 0, 0);                              \
    acc[1][1] = __builtin_amdgcn_mfma_f32_32x32x16_bf16(a1, b1, acc[1][1], 0, 0, 0);                              \
  } while (0)
    B0[0] = FRAG(b0p, 0, 1);
    B1[0] = FRAG(b1p, 0, 1);
    B0[1] = FRAG(b0p, 1, 1);
    B1[1] = FRAG(b1p, 1, 1);
    B0[2] = FRAG(b0p, 2, 1);
    B1[2] = FRAG(b1p, 2, 1);
    B0[3] = FRAG(b0p, 3, 1);
    B1[3] = FRAG(b1p, 3, 1);
    B0[4] = FRAG(b0p, 4, 1);
    B1[4] = FRAG(b1p, 4, 1);
    B0[5] = FRAG(b0p, 5, 1);
    B1[5] = FRAG(b1p, 5, 1);
    B0[6] = FRAG(b0p, 6, 1);
    B1[6] = FRAG(b1p, 6, 1);
    B0[7] = FRAG(b0p, 7, 1);
    B1[7] = FRAG(b1p, 7, 1);
    B0[8] = FRAG(b0p, 8, 1);
    B1[8] = FRAG(b1p, 8, 1);
    B0[9] = FRAG(b0p, 9, 1);
    B1[9] = FRAG(b1p, 9, 1);
    B0[10] = FRAG(b0p, 10, 1);
    B1[10] = FRAG(b1p, 10, 1);
    f32x16 acc[2][2] = {};
    asm volatile("s_waitcnt vmcnt(20)" : "+v"(fr[0]), "+v"(fr[1]), "+v"(B0[0]), "+v"(B1[0]));
    MMA2(0);
    asm volatile("s_waitcnt vmcnt(18)" : "+v"(fr[2]), "+v"(fr[3]), "+v"(B0[1]), "+v"(B1[1]));
    MMA2(1);
    asm volatile("s_waitcnt vmcnt(16)" : "+v"(fr[4]), "+v"(fr[5]), "+v"(B0[2]), "+v"(B1[2]));
    MMA2(2);
    asm volatile("s_waitcnt vmcnt(14)" : "+v"(fr[6]), "+v"(fr[7]), "+v"(B0[3]), "+v"(B1[3]));
    MMA2(3);
    asm volatile("s_waitcnt vmcnt(12)" : "+v"(fr[8]), "+v"(fr[9]), "+v"(B0[4]), "+v"(B1[4]));
    MMA2(4);
    asm volatile("s_waitcnt vmcnt(10)" : "+v"(fr[10]), "+v"(fr[11]), "+v"(B0[5]), "+v"(B1[5]));
    MMA2(5);
    asm volatile("s_waitcnt vmcnt(8)" : "+v"(fr[12]), "+v"(fr[13]), "+v"(B0[6]), "+v"(B1[6]));
    MMA2(6);
    asm volatile("s_waitcnt vmcnt(6)" : "+v"(fr[14]), "+v"(fr[15]), "+v"(B0[7]), "+v"(B1[7]));
    MMA2(7);
    asm volatile("s_waitcnt vmcnt(4)" : "+v"(fr[16]), "+v"(fr[17]), "+v"(B0[8]), "+v"(B1[8]));
    MMA2(8);
    asm volatile("s_waitcnt vmcnt(2)" : "+v"(fr[18]), "+v"(fr[19]), "+v"(B0[9]), "+v"(B1[9]));
    MMA2(9);
    asm volatile("s_waitcnt vmcnt(0)" : "+v"(fr[20]), "+v"(fr[21]), "+v"(B0[10]), "+v"(B1[10]));
    if (k11) MMA2(10);
    STAMP(1, 10);
#pragma unroll
    for (int t = 0; t < 4; ++t) {
#pragma unroll
      for (int hf = 0; hf < 2; ++hf) {
        const int ow = 2 * t + hf;
        if (ow != wave) {
          const int sl = (wave - ow - 1) & 7;
          char* dst = smem + (ow * 7 + sl) * 2048 + lane * 16;
          const f32x16& a = acc[t >> 1][t & 1];
          *(f32x4*)(dst) = (f32x4){a[8 * hf], a[8 * hf + 1], a[8 * hf + 2], a[8 * hf + 3]};
          *(f32x4*)(dst + 1024) = (f32x4){a[8 * hf + 4], a[8 * hf + 5], a[8 * hf + 6], a[8 * hf + 7]};
        }
      }
    }
    __syncthreads();
    float fin[8];
#pragma unroll
    for (int t = 0; t < 4; ++t)
#pragma unroll
      for (int hf = 0; hf < 2; ++hf)
        if (2 * t + hf == wave) {
#pragma unroll
          for (int k = 0; k < 8; ++k) fin[k] = acc[t >> 1][t & 1][8 * hf + k];
        }
#pragma unroll
    for (int s = 0; s < 7; ++s) {
      const char* src = smem + (wave * 7 + s) * 2048 + lane * 16;
      const f32x4 v0 = *(const f32x4*)src, v1 = *(const f32x4*)(src + 1024);
      fin[0] += v0[0];
      fin[1] += v0[1];
      fin[2] += v0[2];
      fin[3] += v0[3];
      fin[4] += v1[0];
      fin[5] += v1[1];
      fin[6] += v1[2];
      fin[7] += v1[3];
    }
#pragma unroll
    for (int k = 0; k < 8; ++k) asm volatile("" : "+v"(res[k]));
    u16* op = out + (size_t)lpos * D_ + dbase + 4 * h;
#pragma unroll
    for (int i = 0; i < 2; ++i) {
      u32x2 o;
      o[0] = pack_bf2(bflo(res[4 * i]) + rbf(fin[4 * i]), bflo(res[4 * i + 1]) + rbf(fin[4 * i + 1]));
      o[1] = pack_bf2(bflo(res[4 * i + 2]) + rbf(fin[4 * i + 2]), bflo(res[4 * i + 3]) + rbf(fin[4 * i + 3]));
      *(u32x2*)(op + 8 * i) = o;
    }
  }
  STAMP(1, 15);
  STAMP_FLUSH(1);
}

}

extern "C" void kernel_launch(void* const* d_in, const int* in_sizes, int n_in, void* d_out, int out_size, void* d_ws,
                              size_t ws_size, hipStream_t stream) {
  const u16* x = (const u16*)d_in[0];
  const u16* lar = (const u16*)d_in[1];
  const u16* aim = (const u16*)d_in[2];
  const u16* cre = (const u16*)d_in[3];
  const u16* cim = (const u16*)d_in[4];
  const u16* logdt = (const u16*)d_in[5];
  const u16* dskip = (const u16*)d_in[6];
  const u16* Wg = (const u16*)d_in[8];
  const u16* Wu = (const u16*)d_in[9];
  const u16* Wd = (const u16*)d_in[10];
  char* ws = (char*)d_ws;
  (void)hipFuncSetAttribute((const void*)k12, hipFuncAttributeMaxDynamicSharedMemorySize, K12_LDS);
  (void)hipFuncSetAttribute((const void*)k34, hipFuncAttributeMaxDynamicSharedMemorySize, K34_LDS);
  k12<<<256, 512, K12_LDS, stream>>>(x, lar, aim, cre, cim, logdt, dskip, Wg, Wu, Wd, ws);
  k34<<<256, 512, K34_LDS, stream>>>(ws, (u16*)d_out);
}
```

```cpp
#include <hip/hip_runtime.h>
#include <hip/hip_bf16.h>
#include <stdint.h>

typedef unsigned short u16;
typedef __attribute__((ext_vector_type(8))) _Float16 f16x8;
typedef __attribute__((ext_vector_type(2))) _Float16 f16x2;
typedef __attribute__((ext_vector_type(8))) __bf16 bf16x8;
typedef __attribute__((ext_vector_type(2))) __bf16 bf16x2;
typedef __attribute__((ext_vector_type(4))) short s16x4;
typedef __attribute__((ext_vector_type(8))) short s16x8;
typedef __attribute__((ext_vector_type(4))) float f32x4;
typedef __attribute__((ext_vector_type(2))) float f32x2;
typedef __attribute__((ext_vector_type(16))) float f32x16;
typedef __attribute__((ext_vector_type(4))) unsigned u32x4;
typedef __attribute__((ext_vector_type(2))) unsigned u32x2;
#define LAS __attribute__((address_space(3)))
#define GAS __attribute__((address_space(1)))
typedef GAS unsigned gu32;
typedef GAS unsigned long long gu64;
#define RLX_AGENT __ATOMIC_RELAXED, __HIP_MEMORY_SCOPE_AGENT

#define STAMP_DECL
#define STAMP(kid, slot)
#define STAMPK(i)
#define STAMP_FLUSH(kid)

namespace {

constexpr int L_ = 2048, D_ = 512, N_ = 128, F_ = 1376;
constexpr int KR_LEN = 2112;
constexpr int X0 = 2064;
constexpr int NFT = F_ / 16;
constexpr int NKS1 = D_ / 16;
constexpr int NLT = L_ / 32;

constexpr size_t WS_CTRL = 0;
constexpr size_t WS_FLAG = 4096;
constexpr size_t WS_FLA = WS_FLAG + 256 * 256;
constexpr size_t WS_SG = WS_FLA + 4096;
constexpr size_t WS_XG = WS_SG + L_ * 8;
constexpr size_t WS_HT = WS_XG + (size_t)D_ * L_ * 4;
constexpr size_t WS_CSP = WS_HT + (size_t)22 * D_ * 96 * 2;
constexpr size_t WS_W1 = WS_CSP + (size_t)8 * NFT * 32 * 4;
constexpr size_t WS_W2 = WS_W1 + (size_t)NFT * NKS1 * 1024;
constexpr size_t WS_HM = WS_W2 + (size_t)16 * NFT * 1024;
constexpr size_t WS_END = WS_HM + (size_t)NLT * NFT * 1024;
constexpr unsigned SPIN_CAP = 1u << 18;

__device__ __forceinline__ float bf2f(u16 v) { return __uint_as_float((unsigned)v << 16); }
__device__ __forceinline__ float bflo(unsigned v) { return __uint_as_float(v << 16); }
__device__ __forceinline__ float bfhi(unsigned v) { return __uint_as_float(v & 0xffff0000u); }
__device__ __forceinline__ u16 f2bf(float f) {
  __bf16 b = (__bf16)f;
  return __builtin_bit_cast(u16, b);
}
__device__ __forceinline__ float rbf(float f) { return bf2f(f2bf(f)); }
__device__ __forceinline__ unsigned pack_bf2(float lo, float hi) {
  bf16x2 v;
  v[0] = (__bf16)lo;
  v[1] = (__bf16)hi;
  return __builtin_bit_cast(unsigned, v);
}
__device__ __forceinline__ unsigned pack_h2(float lo, float hi) {
  f16x2 v;
  v[0] = (_Float16)lo;
  v[1] = (_Float16)hi;
  return __builtin_bit_cast(unsigned, v);
}
template <int CTRL, int ROWMASK>
__device__ __forceinline__ float dpp_get(float v) {
  return __int_as_float(__builtin_amdgcn_update_dpp(0, __float_as_int(v), CTRL, ROWMASK, 0xf, true));
}
__device__ __forceinline__ float wave_sum(float v) {
  v += dpp_get<0x111, 0xf>(v);
  v += dpp_get<0x112, 0xf>(v);
  v += dpp_get<0x114, 0xf>(v);
  v += dpp_get<0x118, 0xf>(v);
  v += dpp_get<0x142, 0xa>(v);
  v += dpp_get<0x143, 0xc>(v);
  return __int_as_float(__builtin_amdgcn_readlane(__float_as_int(v), 63));
}
__device__ __forceinline__ float wave_max_nonneg(float v) {
  v = fmaxf(v, dpp_get<0x111, 0xf>(v));
  v = fmaxf(v, dpp_get<0x112, 0xf>(v));
  v = fmaxf(v, dpp_get<0x114, 0xf>(v));
  v = fmaxf(v, dpp_get<0x118, 0xf>(v));
  v = fmaxf(v, dpp_get<0x142, 0xa>(v));
  v = fmaxf(v, dpp_get<0x143, 0xc>(v));
  return __int_as_float(__builtin_amdgcn_readlane(__float_as_int(v), 63));
}
__device__ __forceinline__ unsigned next_epoch(unsigned c) { return c * 1664525u + 1013904223u; }

typedef f32x2 cplx;
__device__ __forceinline__ cplx cmul(cplx a, cplx b) {
  cplx t, c;
  asm("v_pk_mul_f32 %0, %1, %2 op_sel:[1,1] op_sel_hi:[1,0] neg_lo:[1,0]" : "=v"(t) : "v"(a), "v"(b));
  asm("v_pk_fma_f32 %0, %1, %2, %3 op_sel:[0,0,0] op_sel_hi:[0,1,1]" : "=v"(c) : "v"(a), "v"(b), "v"(t));
  return c;
}
__device__ __forceinline__ cplx csqr(cplx a) { return cmul(a, a); }
__device__ __forceinline__ float exp_acc(float x) {
  const float L2E = 1.4426950408889634f, L2E_LO = 1.9259630e-08f;
  const float hi = x * L2E;
  const float lo = fmaf(x, L2E, -hi) + x * L2E_LO;
  const float e = __builtin_amdgcn_exp2f(hi);
  return fmaf(e, lo * 0.6931471805599453f, e);
}
__device__ __forceinline__ s16x4 tr_read(const char* p) {
  return __builtin_amdgcn_ds_read_tr16_b64_v4i16((LAS s16x4*)p);
}
__device__ __forceinline__ u32x4 tr_pair(const char* p0, const char* p1) {
  const s16x4 a = tr_read(p0), b = tr_read(p1);
  const s16x8 v = {a[0], a[1], a[2], a[3], b[0], b[1], b[2], b[3]};
  return __builtin_bit_cast(u32x4, v);
}

struct KIdxTab {
  unsigned short v[KR_LEN];
  unsigned short s3[2][64][8];
  constexpr KIdxTab() : v(), s3() {
    for (int rt = 0; rt < 2; ++rt)
      for (int lane = 0; lane < 64; ++lane)
        for (int ct = 0; ct < 2; ++ct)
          for (int r = 0; r < 4; ++r) {
            const int row = 16 * rt + 4 * (lane >> 4) + r, j = 16 * ct + (lane & 15);
            int ti = 642;
            if (row < 8) ti = row + 8 * j;
            else if (row < 12) ti = 256 + (row - 8) + 4 * j;
            else if (row < 16) ti = 384 + 64 * ((row - 12) >> 1) + ((row - 12) & 1) + 2 * j;
            else if (row < 20) ti = 512 + 32 * (row - 16) + j;
            else if (row == 20 && j == 0) ti = 640;
            s3[rt][lane][4 * ct + r] = (unsigned short)(2 * ti);
          }
    for (int i = 0; i < KR_LEN; ++i) {
      const int j = X0 + 8 - i;
      int idx = 643;
      if (j >= 0 && j < L_) {
        if (j < 256) {
          idx = j;
        } else {
          int sh = 1;
          while ((j >> sh) >= 256) ++sh;
          int m = j >> sh;
          const int rem = j & ((1 << sh) - 1), half = 1 << (sh - 1);
          if (rem > half || (rem == half && (m & 1))) ++m;
          idx = 128 * sh + m;
        }
      }
      v[i] = (unsigned short)(2 * idx);
    }
  }
};
__device__ constexpr KIdxTab kidx_tab{};

constexpr int VW_PITCH = 528;
constexpr int VW_CH = 64 * VW_PITCH;
constexpr int KC_STRIDE = 4288;
constexpr int KC_CH = 8 * KC_STRIDE;
constexpr int U_PITCH = 80;
constexpr int U_CH = 96 * U_PITCH;
constexpr int Y_PITCH = 144;
constexpr int Y_BUF = 64 * Y_PITCH;
constexpr int FT_CH = 644 * 2 + 8;
constexpr int K12_KC = 0;
constexpr int K12_US = 2 * KC_CH;
constexpr int K12_FT = K12_US + 2 * U_CH;
constexpr int K12_MISC = K12_FT + 2 * FT_CH;
constexpr int K12_KL = K12_MISC + 32;
constexpr int K12_RT = K12_KL + 2 * KR_LEN * 2;
constexpr int RT_WAVE = 6400;
constexpr int K12_Y = K12_RT;
constexpr int K12_TT2 = K12_RT + 4 * Y_BUF;
constexpr int K12_RT2 = K12_TT2 - 0;
constexpr int K12_LDS = K12_TT2 + 4 * RT_WAVE;

__device__ __forceinline__ void gstore16_sc1(void* sbase, unsigned voff, u32x4 v) {
  asm volatile("global_store_dwordx4 %0, %1, %2 sc1\n\ts_nop 1" : : "v"(voff), "v"(v), "s"(sbase) : "memory");
}
template <int OFF>
__device__ __forceinline__ u32x4 gload16_sc1(const void* sbase, unsigned voff) {
  u32x4 v;
  asm volatile("global_load_dwordx4 %0, %1, %2 offset:%3 sc1" : "=v"(v) : "v"(voff), "s"(sbase), "i"(OFF) : "memory");
  return v;
}
#define WAITV(n) asm volatile("s_waitcnt vmcnt(" #n ")" ::: "memory")

__device__ __forceinline__ void gstore8_sc1(void* sbase, unsigned voff, u32x2 v) {
  asm volatile("global_store_dwordx2 %0, %1, %2 sc1" : : "v"(voff), "v"(v), "s"(sbase) : "memory");
}
__device__ __forceinline__ void retile_load(u32x4 (&v)[4], int u, int lane, const u16* __restrict__ Wg,
                                            const u16* __restrict__ Wu, const u16* __restrict__ Wd) {
  if (u < 688) {
    const int kb = u / 86, T = u - kb * 86;
#pragma unroll
    for (int i = 0; i < 4; ++i) {
      const int c = lane + 64 * (i & 1);
      v[i] = *(const u32x4*)(((i >> 1) ? Wu : Wg) + (size_t)(64 * kb + (c >> 1)) * F_ + 16 * T + 8 * (c & 1));
    }
  } else {
    const int u2 = u - 688, ks = u2 >> 2, dq = u2 & 3;
#pragma unroll
    for (int i = 0; i < 4; ++i) {
      const int c = lane + 64 * i;
      v[i] = *(const u32x4*)(Wd + (size_t)(16 * ks + (c >> 4)) * D_ + 128 * dq + 8 * (c & 15));
    }
  }
}
__device__ __forceinline__ void retile_prep(char* rt, u32x4 (&v)[4], float& cs, int u, int lane) {
  const int h = lane >> 5, g = lane >> 4, q = (lane & 15) >> 2, p = lane & 3;
  if (u < 688) {
#pragma unroll
    for (int i = 0; i < 4; ++i) {
      const int c = lane + 64 * (i & 1);
      *(u32x4*)(rt + (i >> 1) * 3104 + (c >> 1) * 48 + (c & 1) * 16) = v[i];
    }
    const char* base = rt + (g & 1) * 3104 + (8 * h + q) * 48 + (8 * (p & 1) + 4 * (p >> 1)) * 2;
#pragma unroll
    for (int kk = 0; kk < 4; ++kk) {
      const char* a = base + kk * 16 * 48;
      v[kk] = tr_pair(a, a + 4 * 48);
    }
    f32x2 sacc = {0.f, 0.f};
#pragma unroll
    for (int kk = 0; kk < 4; ++kk)
#pragma unroll
      for (int k = 0; k < 4; ++k) sacc += (f32x2){bflo(v[kk][k]), bfhi(v[kk][k])};
    const float sv = sacc[0] + sacc[1];
    const auto sw = __builtin_amdgcn_permlane32_swap(__float_as_uint(sv), __float_as_uint(sv), false, false);
    cs = __uint_as_float(sw[0]) + __uint_as_float(sw[1]);
  } else {
#pragma unroll
    for (int i = 0; i < 4; ++i) {
      const int c = lane + 64 * i;
      *(u32x4*)(rt + (c >> 4) * 320 + (c & 15) * 16) = v[i];
    }
    const char* base = rt + (8 * h + q) * 320 + (16 * (g & 1) + 4 * p) * 2;
#pragma unroll
    for (int fr = 0; fr < 4; ++fr) {
      const char* a = base + fr * 64;
      v[fr] = tr_pair(a, a + 4 * 320);
    }
    cs = 0.f;
  }
}
__device__ __forceinline__ void retile_emit(const u32x4 (&v)[4], float cs, int u, int lane, char* __restrict__ ws) {
  if (u < 688) {
    const int kb = u / 86, T = u - kb * 86;
    char* dst = ws + WS_W1 + ((size_t)T * NKS1 + 4 * kb) * 1024;
#pragma unroll
    for (int kk = 0; kk < 4; ++kk) gstore16_sc1(dst + kk * 1024, lane * 16, v[kk]);
    if (lane < 32) __hip_atomic_store((gu32*)(ws + WS_CSP) + ((size_t)T * 8 + kb) * 32 + lane, __float_as_uint(cs), RLX_AGENT);
  } else {
    const int u2 = u - 688, ks = u2 >> 2, dq = u2 & 3;
    char* dst = ws + WS_W2 + ((size_t)(4 * dq) * NFT + ks) * 1024;
#pragma unroll
    for (int fr = 0; fr < 4; ++fr) gstore16_sc1(dst + (size_t)fr * NFT * 1024, lane * 16, v[fr]);
  }
}

constexpr int K3_RB = 22, K3_CG = 11, K3_TILES = K3_RB * K3_CG;
constexpr int K3_APITCH = 192;
constexpr int K3_ACT = D_ * K3_APITCH;
constexpr int K3_ST = K3_ACT;
constexpr int K3_CSX = K3_ST + 8 * 96 * 8;
constexpr int K34_RED = 8 * 7 * 2048;
constexpr int K34_LDS = K34_RED > (K3_CSX + 1024) ? K34_RED : (K3_CSX + 1024);

__device__ __forceinline__ void glds16(const void* sbase, unsigned voff, unsigned lds_dst) {
  unsigned keep;
  asm volatile(
      "s_mov_b32 %0, m0\n\ts_mov_b32 m0, %3\n\ts_nop 0\n\tglobal_load_lds_dwordx4 %1, %2 sc1\n\ts_mov_b32 m0, %0"
      : "=&s"(keep)
      : "v"(voff), "s"(sbase), "s"(lds_dst));
}
template <int OFF, int SC1>
__device__ __forceinline__ u32x4 gld(const void* sbase, unsigned voff) {
  u32x4 v;
  if (SC1)
    asm volatile("global_load_dwordx4 %0, %1, %2 offset:%3 sc1" : "=v"(v) : "v"(voff), "s"(sbase), "i"(OFF));
  else
    asm volatile("global_load_dwordx4 %0, %1, %2 offset:%3" : "=v"(v) : "v"(voff), "s"(sbase), "i"(OFF));
  return v;
}
__device__ __forceinline__ unsigned gld_u32(const void* sbase, unsigned voff) {
  unsigned v;
  asm volatile("global_load_dword %0, %1, %2 sc1" : "=v"(v) : "v"(voff), "s"(sbase));
  return v;
}
__device__ __forceinline__ unsigned gld_u16(const void* sbase, unsigned voff) {
  unsigned v;
  asm volatile("global_load_ushort %0, %1, %2 sc1" : "=v"(v) : "v"(voff), "s"(sbase));
  return v;
}
#define FRAG(base, k, sc1) gld<((k) & 7) * 1024 - 4096, sc1>((base) + ((k) >> 3) * 8192, vo0)
#define WAITR(n, reg) asm volatile("s_waitcnt vmcnt(" #n ")" : "+v"(reg))
#define BARRIER()                    \
  do {                               \
    asm volatile("" ::: "memory");   \
    __builtin_amdgcn_s_barrier();    \
    asm volatile("" ::: "memory");   \
  } while (0)

__global__ __launch_bounds__(512, 2) void kall(const u16* __restrict__ x, const u16* __restrict__ lar,
                                              const u16* __restrict__ aim, const u16* __restrict__ cre,
                                              const u16* __restrict__ cim, const u16* __restrict__ logdt,
                                              const u16* __restrict__ dskip, const u16* __restrict__ Wg,
                                              const u16* __restrict__ Wu, const u16* __restrict__ Wd,
                                              char* __restrict__ ws, u16* __restrict__ out) {
  extern __shared__ __attribute__((aligned(16))) char smem[];
  const int tid = threadIdx.x, lane = tid & 63;
  const int wave = __builtin_amdgcn_readfirstlane(tid >> 6);
  const int b = blockIdx.x;
  gu32* ctrl = (gu32*)(ws + WS_CTRL);
  const unsigned ctrl_old = __hip_atomic_load(ctrl, RLX_AGENT);
  const unsigned epoch = next_epoch(ctrl_old);
  {
  const int cp = (b & 7) * 32 + (b >> 3);
  const int d0 = 2 * cp;
  u16* hT = (u16*)(ws + WS_HT);
  STAMP_DECL;
  STAMP(0, 0);

  const u32x4 rowv = *(const u32x4*)(x + (size_t)(8 * b + wave) * D_ + lane * 8);
  const int kc = wave >> 2, krole = (wave >> 1) & 1, n = (wave & 1) * 64 + lane;
  const int kd = d0 + kc;
  unsigned p_ldt = logdt[kd], p_lar = lar[kd * N_ + n], p_aim = aim[kd * N_ + n];
  unsigned p_c0r = 0, p_c0i = 0, p_c1r = 0, p_c1i = 0;
  if (krole == 0) {
    p_c0r = cre[kd * N_ + lane];
    p_c0i = cim[kd * N_ + lane];
    p_c1r = cre[kd * N_ + 64 + lane];
    p_c1i = cim[kd * N_ + 64 + lane];
  }
  unsigned dsk2 = *(const unsigned*)(dskip + d0);
  u32x4 aux[4];
  const int gw = b * 4 + (wave - 4);
  float rcs = 0.f;
  if (wave < 4) {
    const int t = tid & 127;
    aux[0] = *(const u32x4*)(kidx_tab.v + 8 * t);
    aux[1] = *(const u32x4*)(kidx_tab.v + 8 * (128 + t));
    aux[2] = *(const u32x4*)(kidx_tab.v + 8 * (256 + (t & 7)));
    aux[3] = *(const u32x4*)(kidx_tab.s3[wave & 1][lane]);
  } else {
    retile_load(aux, gw, lane, Wg, Wu, Wd);
  }
  __builtin_amdgcn_sched_barrier(0);
  asm volatile("" : "+v"(p_ldt), "+v"(p_lar), "+v"(p_aim), "+v"(p_c0r), "+v"(p_c0i), "+v"(p_c1r), "+v"(p_c1i), "+v"(dsk2));
  if (wave < 4) asm volatile("" : "+v"(aux[0]), "+v"(aux[1]), "+v"(aux[2]), "+v"(aux[3]));

  {
    char* tt = smem + K12_TT2;
    *(u32x4*)(tt + wave * 1040 + lane * 16) = rowv;
    __syncthreads();
#pragma unroll
    for (int it = 0; it < 2; ++it) {
      const int chn = 64 * wave + 32 * it + (lane >> 1);
      const char* src = tt + (4 * (lane & 1)) * 1040 + chn * 2;
      const unsigned e0 = *(const u16*)src, e1 = *(const u16*)(src + 1040), e2 = *(const u16*)(src + 2080),
                     e3 = *(const u16*)(src + 3120);
      const u32x4 v = {e0 | (e1 << 16), epoch, e2 | (e3 << 16), epoch};
      gstore16_sc1(ws + WS_XG + (size_t)b * 32, chn * (L_ * 4) + (lane & 1) * 16, v);
    }
    float f[8];
#pragma unroll
    for (int k = 0; k < 4; ++k) {
      f[2 * k] = bflo(rowv[k]);
      f[2 * k + 1] = bfhi(rowv[k]);
    }
    const float s = ((f[0] + f[1]) + (f[2] + f[3])) + ((f[4] + f[5]) + (f[6] + f[7]));
    const float mean = wave_sum(s) * (1.f / D_);
    float qv[8];
#pragma unroll
    for (int k = 0; k < 8; ++k) qv[k] = (f[k] - mean) * (f[k] - mean);
    const float var = wave_sum(((qv[0] + qv[1]) + (qv[2] + qv[3])) + ((qv[4] + qv[5]) + (qv[6] + qv[7]))) * (1.f / D_);
    if (lane == 0) {
      const float sd = rbf(__builtin_amdgcn_sqrtf(rbf(rbf(var) + 1e-5f)));
      const unsigned pk = (unsigned)f2bf(mean) | ((unsigned)f2bf(sd) << 16);
      __hip_atomic_store((gu64*)(ws + WS_SG) + (8 * b + wave), ((unsigned long long)epoch << 32) | pk, RLX_AGENT);
    }
  }
  STAMP(0, 1);

  {
    char* Vs = smem + K12_KC + kc * VW_CH;
    char* Ws = Vs + 32 * VW_PITCH;
    const float dt = rbf(exp_acc(bflo(p_ldt)));
    const float ar = -rbf(exp_acc(bflo(p_lar))) * dt;
    const float ai = bflo(p_aim) * dt;
    const float arl = ar * 1.4426950408889634f;
    const float C_HI = 0.15915493667125702f, C_LO = 6.4206383e-09f;
    const float ph = ai * C_HI;
    const float pl = fmaf(ai, C_HI, -ph) + ai * C_LO;
    auto zpow = [&](float t, float sg) -> cplx {
      const float e = __builtin_amdgcn_exp2f(arl * t);
      const float r = __builtin_amdgcn_fractf(ph * t) + pl * t;
      cplx z;
      z[0] = e * __builtin_amdgcn_cosf(r);
      z[1] = (sg * e) * __builtin_amdgcn_sinf(r);
      return z;
    };
    auto pk = [&](cplx v) { return pack_h2(v[0], v[1]); };
    if (krole == 1) {
      cplx w[4];
      w[0] = (cplx){1.f, 0.f};
      w[1] = zpow(8.f, 1.f);
      w[2] = csqr(w[1]);
      const cplx z32 = csqr(w[2]);
      w[3] = cmul(w[1], w[2]);
      char* wp = Ws + n * 4;
#pragma unroll
      for (int g = 0; g < 8; ++g) {
#pragma unroll
        for (int e = 0; e < 4; ++e) {
          *(unsigned*)(wp + (4 * g + e) * VW_PITCH) = pk(w[e]);
          if (g < 7) w[e] = cmul(w[e], z32);
        }
      }
    } else {
      const float c0r = bflo(p_c0r), c0i = bflo(p_c0i), c1r = bflo(p_c1r), c1i = bflo(p_c1i);
      const float cmax = wave_max_nonneg(fmaxf(fmaxf(fabsf(c0r), fabsf(c0i)), fmaxf(fabsf(c1r), fabsf(c1i))));
      const float csum = wave_sum((fabsf(c0r) + fabsf(c0i)) + (fabsf(c1r) + fabsf(c1i)));
      const float inv = cmax > 0.f ? 256.f / cmax : 0.f;
      cplx c;
      c[0] = ((wave & 1) ? c1r : c0r) * inv;
      c[1] = -((wave & 1) ? c1i : c0i) * inv;
      if ((wave & 1) == 0 && lane == 0) {
        const float B = csum * inv;
        float* misc = (float*)(smem + K12_MISC) + 2 * kc;
        misc[0] = B > 0.f ? 16384.f / B : 0.f;
        misc[1] = 2.f * dt * csum * (1.f / 16384.f);
      }
      char* vp = Vs + n * 4;
      auto put = [&](int row, cplx v) { *(unsigned*)(vp + row * VW_PITCH) = pk(v); };
      const cplx z1 = zpow(1.f, -1.f), z256 = zpow(256.f, -1.f);
      const cplx z2 = csqr(z1), z512 = csqr(z256);
      const cplx z4 = csqr(z2), z1024 = csqr(z512);
      const cplx z3 = cmul(z1, z2), z768 = cmul(z512, z256), z2048 = csqr(z1024);
      const cplx v1 = cmul(c, z1), v2 = cmul(c, z2), v4 = cmul(c, z4), v3 = cmul(c, z3);
      const cplx v1024 = cmul(c, z1024);
      put(0, c);
      put(1, v1);
      put(2, v2);
      put(3, v3);
      put(4, v4);
      put(5, cmul(v4, z1));
      const cplx v6 = cmul(v4, z2);
      put(6, v6);
      put(7, cmul(v4, z3));
      put(8, cmul(c, z256));
      put(9, cmul(v2, z256));
      put(10, cmul(v4, z256));
      put(11, cmul(v6, z256));
      put(12, cmul(c, z512));
      put(13, cmul(v4, z512));
      put(14, cmul(c, z768));
      put(15, cmul(v4, z768));
      put(16, v1024);
      put(17, cmul(v1024, z256));
      put(18, cmul(v1024, z512));
      put(19, cmul(v1024, z768));
      put(20, cmul(c, z2048));
    }
  }
  STAMP(0, 2);
  __syncthreads();
  if (wave < 4) {
    const int c3 = wave >> 1, rt = wave & 1, idx = lane & 15, q = lane >> 4;
    const char* Vs = smem + K12_KC + c3 * VW_CH;
    const char* Ws = Vs + 32 * VW_PITCH;
    _Float16* Ft = (_Float16*)(smem + K12_FT + c3 * FT_CH);
    const char* ap = Vs + (16 * rt + idx) * VW_PITCH + q * 16;
    const char* bp = Ws + idx * VW_PITCH + q * 16;
    f16x8 a[8], b0[8], b1[8];
#pragma unroll
    for (int ks = 0; ks < 8; ++ks) {
      a[ks] = *(const f16x8*)(ap + ks * 64);
      b0[ks] = *(const f16x8*)(bp + ks * 64);
      b1[ks] = *(const f16x8*)(bp + 16 * VW_PITCH + ks * 64);
    }
    const float sK = ((const float*)(smem + K12_MISC))[2 * c3];
    __builtin_amdgcn_sched_barrier(0);
    f32x4 acc2[2] = {};
#pragma unroll
    for (int ks = 0; ks < 8; ++ks) {
      acc2[0] = __builtin_amdgcn_mfma_f32_16x16x32_f16(a[ks], b0[ks], acc2[0], 0, 0, 0);
      acc2[1] = __builtin_amdgcn_mfma_f32_16x16x32_f16(a[ks], b1[ks], acc2[1], 0, 0, 0);
    }
#pragma unroll
    for (int k = 0; k < 8; ++k) {
      const unsigned off = (k & 1) ? (aux[3][k >> 1] >> 16) : (aux[3][k >> 1] & 0xffffu);
      *(_Float16*)((char*)Ft + off) = (_Float16)(acc2[k >> 2][k & 3] * sK);
    }
    if (lane == 0 && rt == 0) Ft[643] = (_Float16)0.f;
  } else {
    retile_prep(smem + K12_RT + (wave - 4) * RT_WAVE, aux, rcs, gw, lane);
  }
  STAMP(0, 3);
  __syncthreads();
  if (wave < 4) {
    const int c = wave >> 1, t = tid & 127;
    const char* Ft = smem + K12_FT + c * FT_CH;
    char* Kl = smem + K12_KL + c * KR_LEN * 2;
    auto expand = [&](u32x4 ix, int ch) {
      u32x4 o;
#pragma unroll
      for (int k = 0; k < 4; ++k) {
        const unsigned lo = *(const u16*)(Ft + (ix[k] & 0xffffu)), hi = *(const u16*)(Ft + (ix[k] >> 16));
        o[k] = lo | (hi << 16);
      }
      *(u32x4*)(Kl + ch * 16) = o;
    };
    expand(aux[0], t);
    expand(aux[1], 128 + t);
    if (t < 8) expand(aux[2], 256 + t);
  } else {
    *(u32x4*)(smem + K12_US + ((tid >> 7) & 1) * U_CH + ((tid & 127) >> 2) * U_PITCH + (tid & 3) * 16) = (u32x4){0u, 0u, 0u, 0u};
  }
  STAMP(0, 4);
  __syncthreads();
  const int mc = wave >> 2, mt = tid & 255;
  const char* gx = ws + WS_XG + (size_t)(d0 + mc) * L_ * 4;
  const unsigned gxo = mt * 32;
  const char* gs = ws + WS_SG;
  u32x4 ga[2], gb[4];
#define GATHER_ISSUE()                                                       \
  do {                                                                       \
    ga[0] = gload16_sc1<0>(gx, gxo);                                         \
    ga[1] = gload16_sc1<16>(gx, gxo);                                        \
    gb[0] = gload16_sc1<0>(gs, mt * 64);                                     \
    gb[1] = gload16_sc1<16>(gs, mt * 64);                                    \
    gb[2] = gload16_sc1<32>(gs, mt * 64);                                    \
    gb[3] = gload16_sc1<48>(gs, mt * 64);                                    \
  } while (0)
  GATHER_ISSUE();
  {
    const int c = tid >> 8, t = tid & 255;
    const char* Kl = smem + K12_KL + c * KR_LEN * 2;
    char* dst = smem + K12_KC + c * KC_CH;
    auto copies = [&](int m) {
      const u32x4 a = *(const u32x4*)(Kl + 16 * m), bq = *(const u32x4*)(Kl + 16 * m + 16);
      const unsigned w[8] = {a[0], a[1], a[2], a[3], bq[0], bq[1], bq[2], bq[3]};
#pragma unroll
      for (int cc = 0; cc < 8; ++cc) {
        const int s = 8 - cc;
        u32x4 o;
        if ((s & 1) == 0) {
#pragma unroll
          for (int k = 0; k < 4; ++k) o[k] = w[s / 2 + k];
        } else {
#pragma unroll
          for (int k = 0; k < 4; ++k) o[k] = __builtin_amdgcn_alignbit(w[(s + 1) / 2 + k], w[(s - 1) / 2 + k], 16);
        }
        *(u32x4*)(dst + cc * KC_STRIDE + m * 16) = o;
      }
    };
    copies(t);
    if (t < 6) copies(256 + t);
  }
  STAMP(0, 5);
  for (unsigned spins = 0;; ++spins) {
    WAITV(0);
    asm volatile("" : "+v"(ga[0]), "+v"(ga[1]), "+v"(gb[0]), "+v"(gb[1]), "+v"(gb[2]), "+v"(gb[3]));
    const bool ok = ga[0][1] == epoch && ga[0][3] == epoch && ga[1][1] == epoch && ga[1][3] == epoch &&
                    gb[0][1] == epoch && gb[0][3] == epoch && gb[1][1] == epoch && gb[1][3] == epoch &&
                    gb[2][1] == epoch && gb[2][3] == epoch && gb[3][1] == epoch && gb[3][3] == epoch;
    if (__all(ok) || spins > SPIN_CAP) break;
    __builtin_amdgcn_s_sleep(1);
    GATHER_ISSUE();
  }
  unsigned xw[4], uw[4];
#pragma unroll
  for (int k = 0; k < 4; ++k) {
    xw[k] = ga[k >> 1][2 * (k & 1)];
    const unsigned s0 = gb[k][0], s1 = gb[k][2];
    const float u0 = rbf(rbf(bflo(xw[k]) - bflo(s0)) * __builtin_amdgcn_rcpf(bfhi(s0)));
    const float u1 = rbf(rbf(bfhi(xw[k]) - bflo(s1)) * __builtin_amdgcn_rcpf(bfhi(s1)));
    uw[k] = pack_h2(u0, u1);
  }
  *(u32x4*)(smem + K12_US + mc * U_CH + (32 + (mt >> 2)) * U_PITCH + (mt & 3) * 16) = (u32x4){uw[0], uw[1], uw[2], uw[3]};
  STAMP(0, 6);
  __syncthreads();
  if (b == 0 && tid == 0) __hip_atomic_store(ctrl, epoch, RLX_AGENT);
  STAMP(0, 7);

  const int r = lane & 31, h = lane >> 5;
  if (wave < 4) {
    f32x16 acc0 = {}, acc1 = {};
    const int ch = wave >> 1, w2 = wave & 1;
    const char* abase = smem + K12_KC + ch * KC_CH + (r & 7) * KC_STRIDE + (X0 - 8 * (r >> 3) + 8 * h) * 2 - 64 * 16 * w2;
    const char* bbase = smem + K12_US + ch * U_CH + (r + 32) * U_PITCH + h * 16 - 16 * w2 * U_PITCH;
    f16x8 A1[4], A2[4], B1[4], B2[4];
#define CONV_LOAD(it_)                                                           \
  do {                                                                           \
    const int dd_ = (it_) >> 1, hp_ = (it_) & 1, s_ = (it_) & 3;                 \
    A1[s_] = *(const f16x8*)(abase - (2 * dd_ - hp_) * 32);                      \
    B1[s_] = *(const f16x8*)(bbase - dd_ * U_PITCH + hp_ * 32);                  \
    B2[s_] = *(const f16x8*)(bbase + (32 - dd_) * U_PITCH + hp_ * 32);           \
    A2[s_] = *(const f16x8*)(abase - (2 * dd_ - hp_ + 64) * 32);                 \
  } while (0)
    CONV_LOAD(0);
    CONV_LOAD(1);
    CONV_LOAD(2);
#pragma unroll
    for (int it = 0; it < 32; ++it) {
      if (it + 3 < 32) CONV_LOAD(it + 3);
      __builtin_amdgcn_sched_barrier(0);
      const int s = it & 3;
      acc0 = __builtin_amdgcn_mfma_f32_32x32x16_f16(A1[s], B1[s], acc0, 0, 0, 0);
      acc1 = __builtin_amdgcn_mfma_f32_32x32x16_f16(A1[s], B2[s], acc1, 0, 0, 0);
      acc1 = __builtin_amdgcn_mfma_f32_32x32x16_f16(A2[s], B1[s], acc1, 0, 0, 0);
      __builtin_amdgcn_sched_barrier(0);
    }
    STAMP(0, 8);
    char* yb = smem + K12_Y + wave * Y_BUF;
#pragma unroll
    for (int i = 0; i < 4; ++i) {
      *(f32x4*)(yb + r * Y_PITCH + (8 * i + 4 * h) * 4) =
          (f32x4){acc0[4 * i], acc0[4 * i + 1], acc0[4 * i + 2], acc0[4 * i + 3]};
      *(f32x4*)(yb + (32 + r) * Y_PITCH + (8 * i + 4 * h) * 4) =
          (f32x4){acc1[4 * i], acc1[4 * i + 1], acc1[4 * i + 2], acc1[4 * i + 3]};
    }
  } else {
    retile_emit(aux, rcs, gw, lane, ws);
    if (gw < 8) {
      retile_load(aux, 1024 + gw, lane, Wg, Wu, Wd);
      retile_prep(smem + K12_RT2 + (wave - 4) * RT_WAVE, aux, rcs, 1024 + gw, lane);
      retile_emit(aux, rcs, 1024 + gw, lane, ws);
    }
    WAITV(0);
    STAMP(0, 8);
  }
  __syncthreads();
  if (tid == 0) __hip_atomic_store((gu32*)(ws + WS_FLA) + 256 + b, epoch, RLX_AGENT);
  STAMP(0, 9);
  {
    const int lp = 8 * mt, rbl = lp / 96;
    const char* y0 = smem + K12_Y + (mc * 2) * Y_BUF + (mt >> 2) * Y_PITCH + (mt & 3) * 32;
    const f32x4 p0 = *(const f32x4*)y0, p1 = *(const f32x4*)(y0 + 16);
    const f32x4 q0 = *(const f32x4*)(y0 + Y_BUF), q1 = *(const f32x4*)(y0 + Y_BUF + 16);
    const float ks = ((const float*)(smem + K12_MISC))[2 * mc + 1];
    const float dsk = mc ? bfhi(dsk2) : bflo(dsk2);
    float hv[8];
#pragma unroll
    for (int k = 0; k < 8; ++k) {
      const float y = rbf((k < 4 ? p0[k & 3] + q0[k & 3] : p1[k & 3] + q1[k & 3]) * ks);
      const unsigned uh = (k & 1) ? (uw[k >> 1] >> 16) : (uw[k >> 1] & 0xffffu);
      const float uu = (float)__builtin_bit_cast(_Float16, (u16)uh);
      const float t = rbf(uu * dsk);
      const float a = rbf(y + t);
      hv[k] = ((k & 1) ? bfhi(xw[k >> 1]) : bflo(xw[k >> 1])) + a;
    }
    const u32x4 o = {pack_bf2(hv[0], hv[1]), pack_bf2(hv[2], hv[3]), pack_bf2(hv[4], hv[5]), pack_bf2(hv[6], hv[7])};
    const unsigned o1 = lp < 2016 ? ((rbl * D_ + d0 + mc) * 96 + (lp - 96 * rbl)) * 2 : ((21 * D_ + d0 + mc) * 96 + (lp - 1952)) * 2;
    gstore16_sc1(hT, o1, o);
    if (lp >= 1952 && lp < 2016) gstore16_sc1(hT, ((21 * D_ + d0 + mc) * 96 + (lp - 1952)) * 2, o);
  }
  STAMP(0, 15);
  WAITV(0);
  __syncthreads();
  if (tid == 0) __hip_atomic_store((gu32*)(ws + WS_FLA) + b, epoch, RLX_AGENT);
  STAMP_FLUSH(0);
  }

  {
  const u16* hT = (const u16*)(ws + WS_HT);
  gu32* flags = (gu32*)(ws + WS_FLAG);
  const unsigned lds0 = (unsigned)(size_t)(LAS char*)smem;
  const int r = lane & 31, h = lane >> 5;
  const unsigned lane16 = lane * 16;
  unsigned vo0 = lane16 + 4096;
  const int xcd = blockIdx.x & 7, slot = blockIdx.x >> 3;
  const int rb2 = 4 * xcd + (slot >> 3), cb = slot & 7;
  const int ks0 = wave < 6 ? wave * 11 : 66 + (wave - 6) * 10;
  const bool k11 = wave < 6;
  const int own = wave >> 1, half = wave & 1;
  const int dbase = cb * 64 + (own >> 1) * 32 + 16 * half, lpos = rb2 * 64 + (own & 1) * 32 + r;
  u32x4 fr[32];
  unsigned res[8];
  STAMP_DECL;
  STAMP(1, 0);
#define LOAD_RES()                                                                                         \
  do {                                                                                                     \
    const int rbl = min(lpos / 96, 21), lo = lpos - (rbl == 21 ? 1952 : 96 * rbl);                         \
    const unsigned ro = ((rbl * D_ + dbase + 4 * h) * 96 + lo) * 2;                                        \
    _Pragma("unroll") for (int k = 0; k < 8; ++k) res[k] =                                                 \
        gld_u16((const char*)hT + (8 * (k >> 2) + (k & 3)) * 192, ro);                                     \
  } while (0)
#define WAIT_FLAGS(off)                                                                        \
  do {                                                                                         \
    if (wave == 0) {                                                                           \
      const gu32* fa = (const gu32*)(ws + WS_FLA) + (off);                                     \
      for (unsigned spins = 0;; ++spins) {                                                     \
        bool ok = true;                                                                        \
        _Pragma("unroll") for (int i = 0; i < 4; ++i) ok &=                                    \
            __hip_atomic_load(fa + lane + 64 * i, RLX_AGENT) == epoch;                         \
        if (__all(ok) || spins > SPIN_CAP) break;                                              \
        __builtin_amdgcn_s_sleep(1);                                                           \
      }                                                                                        \
    }                                                                                          \
    __syncthreads();                                                                           \
  } while (0)
  WAIT_FLAGS(256);
  STAMP(1, 11);

  const int p1 = 30 * xcd + min(xcd, 2) + slot;
  const bool has1 = slot < (xcd < 2 ? 31 : 30);
  const char* a0p = ws + WS_W2 + ((size_t)(2 * cb) * NFT + ks0) * 1024;
  const char* a1p = a0p + (size_t)NFT * 1024;
  if (has1) {
    int rb, cg;
    if (p1 < 132) {
      rb = p1 / 6;
      cg = p1 - rb * 6;
    } else {
      const int p2 = p1 - 132;
      rb = p2 / 5;
      cg = 6 + p2 - rb * 5;
    }
    const int l0 = min(rb * 96, L_ - 96);
    const int Treal = cg * 8 + wave;
    const int T = min(Treal, NFT - 1);
    const char* wp = ws + WS_W1 + (size_t)T * NKS1 * 1024;
    const char* hp = (const char*)hT + (size_t)rb * (D_ * 192) + 3 * wave * 1024;
    const unsigned ldsw = lds0 + 3 * wave * 1024;
    unsigned csp[4];
#define LOAD_CSP()                                                                      \
  do {                                                                                  \
    const char* cp_ = ws + WS_CSP + (size_t)T * 1024;                                   \
    const unsigned co = h * 512 + r * 4;                                                \
    _Pragma("unroll") for (int k = 0; k < 4; ++k) csp[k] = gld_u32(cp_ + k * 128, co);  \
  } while (0)
    const int blk = (lane >> 4) & 1, q = (lane & 15) >> 2, p = lane & 3;
    const char* tb = smem + (8 * h + q) * K3_APITCH + (16 * blk + 4 * p) * 2;
    f32x16 acc[3] = {};
    float ssum[3] = {0.f, 0.f, 0.f}, ssq[3] = {0.f, 0.f, 0.f}, ssumb[3] = {0.f, 0.f, 0.f}, ssqb[3] = {0.f, 0.f, 0.f};
    u32x4 bq[6];
    f32x2* sp = (f32x2*)(smem + K3_ST);
    float* csx = (float*)(smem + K3_CSX) + wave * 32;
    float cs[16], mb[3], rs2[3];
#define BREAD(buf, ks, lt)                                                            \
  do {                                                                                \
    const char* bp_ = tb + (ks) * 16 * K3_APITCH + (lt) * 64;                         \
    bq[buf] = tr_pair(bp_, bp_ + 4 * K3_APITCH);                                      \
  } while (0)
#define MMA1(buf, ks, lt)                                                                                  \
  do {                                                                                                     \
    acc[lt] = __builtin_amdgcn_mfma_f32_32x32x16_bf16(__builtin_bit_cast(bf16x8, fr[ks]),                  \
                                                      __builtin_bit_cast(bf16x8, bq[buf]), acc[lt], 0, 0, 0); \
    asm volatile("" : "+v"(acc[lt]));              \
  } while (0)
#define EPI(lt)                                                                       \
  do {                                                                                \
    float hv[8];                                                                      \
    _Pragma("unroll") for (int k = 0; k < 8; ++k) {                                   \
      const float g = fmaf(-mb[lt], cs[k], acc[lt][k]);                               \
      const float u = fmaf(-mb[lt], cs[8 + k], acc[lt][8 + k]);                       \
      hv[k] = fmaxf(g, 0.f) * rs2[lt] * u;                                            \
    }                                                                                 \
    u32x4 o;                                                                          \
    _Pragma("unroll") for (int k = 0; k < 4; ++k) o[k] = pack_bf2(hv[2 * k], hv[2 * k + 1]); \
    gstore16_sc1(hmp + (size_t)(lt) * NFT * 1024, lane16, o);                         \
  } while (0)
    char* hmp = Treal < NFT ? ws + WS_HM + ((size_t)(l0 / 32) * NFT + T) * 1024 : ws + WS_END + 65536;
    u32x4 sq;
    auto stats_issue = [&](int c, int lt) {
      const char* s0 = tb + (8 * c + wave) * 16 * K3_APITCH + lt * 64;
      sq = tr_pair(s0, s0 + 4 * K3_APITCH);
    };
    auto stats_sub = [&](int lt, int k) {
      const float lo = bflo(sq[k]), hi = bfhi(sq[k]);
      asm("v_add_f32 %0, %0, %1" : "+v"(ssum[lt]) : "v"(lo));
      asm("v_fmac_f32 %0, %1, %1" : "+v"(ssq[lt]) : "v"(lo));
      asm("v_add_f32 %0, %0, %1" : "+v"(ssumb[lt]) : "v"(hi));
      asm("v_fmac_f32 %0, %1, %1" : "+v"(ssqb[lt]) : "v"(hi));
    };
    auto publish_partials = [&]() {
      const float csl = (__uint_as_float(csp[0]) + __uint_as_float(csp[1])) + (__uint_as_float(csp[2]) + __uint_as_float(csp[3]));
      float v[7] = {ssum[0] + ssumb[0], ssq[0] + ssqb[0], ssum[1] + ssumb[1], ssq[1] + ssqb[1], ssum[2] + ssumb[2], ssq[2] + ssqb[2], csl};
#pragma unroll
      for (int i = 0; i < 7; ++i) {
        const auto sw = __builtin_amdgcn_permlane32_swap(__float_as_uint(v[i]), __float_as_uint(v[i]), false, false);
        v[i] = __uint_as_float(sw[0]) + __uint_as_float(sw[1]);
      }
      sp[wave * 96 + h * 32 + r] = h ? (f32x2){v[2], v[3]} : (f32x2){v[0], v[1]};
      if (h == 0) sp[wave * 96 + 64 + r] = (f32x2){v[4], v[5]};
      else csx[r] = v[6];
    };
    float m2[2], q2[2];
    f32x2 fv[8];
    auto finish_issue = [&](int rd) {
      const int li = rd ? 64 + r : lane;
#pragma unroll
      for (int w = 0; w < 8; ++w) fv[w] = sp[w * 96 + li];
    };
    auto finish_round = [&](int rd) {
      const f32x2 tv = ((fv[0] + fv[1]) + (fv[2] + fv[3])) + ((fv[4] + fv[5]) + (fv[6] + fv[7]));
      const float mean = tv[0] * (1.f / D_);
      const float var = fmaxf(tv[1] * (1.f / D_) - mean * mean, 0.f);
      const float sd = rbf(__builtin_amdgcn_sqrtf(rbf(rbf(var) + 1e-5f)));
      const float rs = __builtin_amdgcn_rcpf(sd);
      m2[rd] = rbf(mean);
      q2[rd] = rs * rs;
    };
    auto finish_rest = [&]() {
#pragma unroll
      for (int i = 0; i < 4; ++i) {
        const f32x4 v = *(const f32x4*)(csx + 8 * i + 4 * h);
        cs[4 * i] = v[0];
        cs[4 * i + 1] = v[1];
        cs[4 * i + 2] = v[2];
        cs[4 * i + 3] = v[3];
      }
      const auto sm = __builtin_amdgcn_permlane32_swap(__float_as_uint(m2[0]), __float_as_uint(m2[0]), false, false);
      const auto sq = __builtin_amdgcn_permlane32_swap(__float_as_uint(q2[0]), __float_as_uint(q2[0]), false, false);
      mb[0] = __uint_as_float(sm[0]);
      mb[1] = __uint_as_float(sm[1]);
      rs2[0] = __uint_as_float(sq[0]);
      rs2[1] = __uint_as_float(sq[1]);
      mb[2] = m2[1];
      rs2[2] = q2[1];
    };

    fr[0] = FRAG(wp, 0, 1);
    fr[1] = FRAG(wp, 1, 1);
    fr[2] = FRAG(wp, 2, 1);
    fr[3] = FRAG(wp, 3, 1);
    fr[4] = FRAG(wp, 4, 1);
    fr[5] = FRAG(wp, 5, 1);
    fr[6] = FRAG(wp, 6, 1);
    fr[7] = FRAG(wp, 7, 1);
    fr[8] = FRAG(wp, 8, 1);
    fr[9] = FRAG(wp, 9, 1);
    fr[10] = FRAG(wp, 10, 1);
    fr[11] = FRAG(wp, 11, 1);
    fr[12] = FRAG(wp, 12, 1);
    fr[13] = FRAG(wp, 13, 1);
    fr[14] = FRAG(wp, 14, 1);
    fr[15] = FRAG(wp, 15, 1);
    STAMP(1, 12);
    WAIT_FLAGS(0);
    STAMP(1, 13);
    glds16(hp + 0, lane16, ldsw + 0);
    glds16(hp + 1024, lane16, ldsw + 1024);
    glds16(hp + 2048, lane16, ldsw + 2048);
    glds16(hp + 24576, lane16, ldsw + 24576);
    glds16(hp + 25600, lane16, ldsw + 25600);
    glds16(hp + 26624, lane16, ldsw + 26624);
    glds16(hp + 49152, lane16, ldsw + 49152);
    glds16(hp + 50176, lane16, ldsw + 50176);
    glds16(hp + 51200, lane16, ldsw + 51200);
    glds16(hp + 73728, lane16, ldsw + 73728);
    glds16(hp + 74752, lane16, ldsw + 74752);
    glds16(hp + 75776, lane16, ldsw + 75776);
    STAMP(1, 1);
    WAITR(9, vo0);
    BARRIER();
    STAMP(1, 2);
    BREAD(0, 0, 0);
    BREAD(1, 0, 1);
    BREAD(2, 0, 2);
    BREAD(3, 1, 0);
    BREAD(4, 1, 1);
    BREAD(5, 1, 2);
    WAITR(26, fr[1]);
    asm volatile("" : "+v"(fr[0]));
    MMA1(0, 0, 0);
    __builtin_amdgcn_sched_barrier(0);
    __builtin_amdgcn_sched_barrier(0);
    MMA1(1, 0, 1);
    __builtin_amdgcn_sched_barrier(0);
    __builtin_amdgcn_sched_barrier(0);
    MMA1(2, 0, 2);
    __builtin_amdgcn_sched_barrier(0);
    stats_issue(0, 0);
    fr[16] = FRAG(wp, 16, 1);
    __builtin_amdgcn_sched_barrier(0);
    BREAD(0, 2, 0);
    BREAD(1, 2, 1);
    BREAD(2, 2, 2);
    MMA1(3, 1, 0);
    __builtin_amdgcn_sched_barrier(0);
    stats_sub(0, 0);
    __builtin_amdgcn_sched_barrier(0);
    MMA1(4, 1, 1);
    __builtin_amdgcn_sched_barrier(0);
    stats_sub(0, 1);
    __builtin_amdgcn_sched_barrier(0);
    MMA1(5, 1, 2);
    __builtin_amdgcn_sched_barrier(0);
    fr[17] = FRAG(wp, 17, 1);
    __builtin_amdgcn_sched_barrier(0);
    BREAD(3, 3, 0);
    BREAD(4, 3, 1);
    BREAD(5, 3, 2);
    WAITR(26, fr[3]);
    asm volatile("" : "+v"(fr[2]));
    MMA1(0, 2, 0);
    __builtin_amdgcn_sched_barrier(0);
    stats_sub(0, 2);
    __builtin_amdgcn_sched_barrier(0);
    MMA1(1, 2, 1);
    __builtin_amdgcn_sched_barrier(0);
    stats_sub(0, 3);
    __builtin_amdgcn_sched_barrier(0);
    MMA1(2, 2, 2);
    __builtin_amdgcn_sched_barrier(0);
    stats_issue(0, 1);
    fr[18] = FRAG(wp, 18, 1);
    __builtin_amdgcn_sched_barrier(0);
    BREAD(0, 4, 0);
    BREAD(1, 4, 1);
    BREAD(2, 4, 2);
    MMA1(3, 3, 0);
    __builtin_amdgcn_sched_barrier(0);
    stats_sub(1, 0);
    __builtin_amdgcn_sched_barrier(0);
    MMA1(4, 3, 1);
    __builtin_amdgcn_sched_barrier(0);
    stats_sub(1, 1);
    __builtin_amdgcn_sched_barrier(0);
    MMA1(5, 3, 2);
    __builtin_amdgcn_sched_barrier(0);
    fr[19] = FRAG(wp, 19, 1);
    __builtin_amdgcn_sched_barrier(0);
    BREAD(3, 5, 0);
    BREAD(4, 5, 1);
    BREAD(5, 5, 2);
    WAITR(26, fr[5]);
    asm volatile("" : "+v"(fr[4]));
    MMA1(0, 4, 0);
    __builtin_amdgcn_sched_barrier(0);
    stats_sub(1, 2);
    __builtin_amdgcn_sched_barrier(0);
    MMA1(1, 4, 1);
    __builtin_amdgcn_sched_barrier(0);
    stats_sub(1, 3);
    __builtin_amdgcn_sched_barrier(0);
    MMA1(2, 4, 2);
    __builtin_amdgcn_sched_barrier(0);
    stats_issue(0, 2);
    fr[20] = FRAG(wp, 20, 1);
    __builtin_amdgcn_sched_barrier(0);
    BREAD(0, 6, 0);
    BREAD(1, 6, 1);
    BREAD(2, 6, 2);
    MMA1(3, 5, 0);
    __builtin_amdgcn_sched_barrier(0);
    stats_sub(2, 0);
    __builtin_amdgcn_sched_barrier(0);
    MMA1(4, 5, 1);
    __builtin_amdgcn_sched_barrier(0);
    stats_sub(2, 1);
    __builtin_amdgcn_sched_barrier(0);
    MMA1(5, 5, 2);
    __builtin_amdgcn_sched_barrier(0);
    fr[21] = FRAG(wp, 21, 1);
    __builtin_amdgcn_sched_barrier(0);
    BREAD(3, 7, 0);
    BREAD(4, 7, 1);
    BREAD(5, 7, 2);
    WAITR(26, fr[7]);
    asm volatile("" : "+v"(fr[6]));
    MMA1(0, 6, 0);
    __builtin_amdgcn_sched_barrier(0);
    stats_sub(2, 2);
    __builtin_amdgcn_sched_barrier(0);
    MMA1(1, 6, 1);
    __builtin_amdgcn_sched_barrier(0);
    stats_sub(2, 3);
    __builtin_amdgcn_sched_barrier(0);
    MMA1(2, 6, 2);
    __builtin_amdgcn_sched_barrier(0);
    fr[22] = FRAG(wp, 22, 1);
    __builtin_amdgcn_sched_barrier(0);
    WAITR(13, vo0);
    BARRIER();
    BREAD(0, 8, 0);
    BREAD(1, 8, 1);
    BREAD(2, 8, 2);
    MMA1(3, 7, 0);
    __builtin_amdgcn_sched_barrier(0);
    __builtin_amdgcn_sched_barrier(0);
    MMA1(4, 7, 1);
    __builtin_amdgcn_sched_barrier(0);
    __builtin_amdgcn_sched_barrier(0);
    MMA1(5, 7, 2);
    __builtin_amdgcn_sched_barrier(0);
    stats_issue(1, 0);
    fr[23] = FRAG(wp, 23, 1);
    __builtin_amdgcn_sched_barrier(0);
    STAMP(1, 3);
    BREAD(3, 9, 0);
    BREAD(4, 9, 1);
    BREAD(5, 9, 2);
    WAITR(26, fr[9]);
    asm volatile("" : "+v"(fr[8]));
    MMA1(0, 8, 0);
    __builtin_amdgcn_sched_barrier(0);
    stats_sub(0, 0);
    __builtin_amdgcn_sched_barrier(0);
    MMA1(1, 8, 1);
    __builtin_amdgcn_sched_barrier(0);
    stats_sub(0, 1);
    __builtin_amdgcn_sched_barrier(0);
    MMA1(2, 8, 2);
    __builtin_amdgcn_sched_barrier(0);
    fr[24] = FRAG(wp, 24, 1);
    __builtin_amdgcn_sched_barrier(0);
    BREAD(0, 10, 0);
    BREAD(1, 10, 1);
    BREAD(2, 10, 2);
    MMA1(3, 9, 0);
    __builtin_amdgcn_sched_barrier(0);
    stats_sub(0, 2);
    __builtin_amdgcn_sched_barrier(0);
    MMA1(4, 9, 1);
    __builtin_amdgcn_sched_barrier(0);
    stats_sub(0, 3);
    __builtin_amdgcn_sched_barrier(0);
    MMA1(5, 9, 2);
    __builtin_amdgcn_sched_barrier(0);
    stats_issue(1, 1);
    fr[25] = FRAG(wp, 25, 1);
    __builtin_amdgcn_sched_barrier(0);
    BREAD(3, 11, 0);
    BREAD(4, 11, 1);
    BREAD(5, 11, 2);
    WAITR(26, fr[11]);
    asm volatile("" : "+v"(fr[10]));
    MMA1(0, 10, 0);
    __builtin_amdgcn_sched_barrier(0);
    stats_sub(1, 0);
    __builtin_amdgcn_sched_barrier(0);
    MMA1(1, 10, 1);
    __builtin_amdgcn_sched_barrier(0);
    stats_sub(1, 1);
    __builtin_amdgcn_sched_barrier(0);
    MMA1(2, 10, 2);
    __builtin_amdgcn_sched_barrier(0);
    fr[26] = FRAG(wp, 26, 1);
    __builtin_amdgcn_sched_barrier(0);
    BREAD(0, 12, 0);
    BREAD(1, 12, 1);
    BREAD(2, 12, 2);
    MMA1(3, 11, 0);
    __builtin_amdgcn_sched_barrier(0);
    stats_sub(1, 2);
    __builtin_amdgcn_sched_barrier(0);
    MMA1(4, 11, 1);
    __builtin_amdgcn_sched_barrier(0);
    stats_sub(1, 3);
    __builtin_amdgcn_sched_barrier(0);
    MMA1(5, 11, 2);
    __builtin_amdgcn_sched_barrier(0);
    stats_issue(1, 2);
    fr[27] = FRAG(wp, 27, 1);
    __builtin_amdgcn_sched_barrier(0);
    BREAD(3, 13, 0);
    BREAD(4, 13, 1);
    BREAD(5, 13, 2);
    WAITR(26, fr[13]);
    asm volatile("" : "+v"(fr[12]));
    MMA1(0, 12, 0);
    __builtin_amdgcn_sched_barrier(0);
    stats_sub(2, 0);
    __builtin_amdgcn_sched_barrier(0);
    MMA1(1, 12, 1);
    __builtin_amdgcn_sched_barrier(0);
    stats_sub(2, 1);
    __builtin_amdgcn_sched_barrier(0);
    MMA1(2, 12, 2);
    __builtin_amdgcn_sched_barrier(0);
    fr[28] = FRAG(wp, 28, 1);
    __builtin_amdgcn_sched_barrier(0);
    BREAD(0, 14, 0);
    BREAD(1, 14, 1);
    BREAD(2, 14, 2);
    MMA1(3, 13, 0);
    __builtin_amdgcn_sched_barrier(0);
    stats_sub(2, 2);
    __builtin_amdgcn_sched_barrier(0);
    MMA1(4, 13, 1);
    __builtin_amdgcn_sched_barrier(0);
    stats_sub(2, 3);
    __builtin_amdgcn_sched_barrier(0);
    MMA1(5, 13, 2);
    __builtin_amdgcn_sched_barrier(0);
    fr[29] = FRAG(wp, 29, 1);
    __builtin_amdgcn_sched_barrier(0);
    BREAD(3, 15, 0);
    BREAD(4, 15, 1);
    BREAD(5, 15, 2);
    WAITR(26, fr[15]);
    asm volatile("" : "+v"(fr[14]));
    MMA1(0, 14, 0);
    __builtin_amdgcn_sched_barrier(0);
    __builtin_amdgcn_sched_barrier(0);
    MMA1(1, 14, 1);
    __builtin_amdgcn_sched_barrier(0);
    __builtin_amdgcn_sched_barrier(0);
    MMA1(2, 14, 2);
    __builtin_amdgcn_sched_barrier(0);
    fr[30] = FRAG(wp, 30, 1);
    __builtin_amdgcn_sched_barrier(0);
    WAITR(18, vo0);
    BARRIER();
    BREAD(0, 16, 0);
    BREAD(1, 16, 1);
    BREAD(2, 16, 2);
    MMA1(3, 15, 0);
    __builtin_amdgcn_sched_barrier(0);
    __builtin_amdgcn_sched_barrier(0);
    MMA1(4, 15, 1);
    __builtin_amdgcn_sched_barrier(0);
    __builtin_amdgcn_sched_barrier(0);
    MMA1(5, 15, 2);
    __builtin_amdgcn_sched_barrier(0);
    stats_issue(2, 0);
    fr[31] = FRAG(wp, 31, 1);
    __builtin_amdgcn_sched_barrier(0);
    STAMP(1, 4);
    BREAD(3, 17, 0);
    BREAD(4, 17, 1);
    BREAD(5, 17, 2);
    WAITR(14, fr[17]);
    asm volatile("" : "+v"(fr[16]));
    MMA1(0, 16, 0);
    __builtin_amdgcn_sched_barrier(0);
    stats_sub(0, 0);
    __builtin_amdgcn_sched_barrier(0);
    MMA1(1, 16, 1);
    __builtin_amdgcn_sched_barrier(0);
    stats_sub(0, 1);
    __builtin_amdgcn_sched_barrier(0);
    MMA1(2, 16, 2);
    __builtin_amdgcn_sched_barrier(0);
    LOAD_CSP();
    __builtin_amdgcn_sched_barrier(0);
    BREAD(0, 18, 0);
    BREAD(1, 18, 1);
    BREAD(2, 18, 2);
    MMA1(3, 17, 0);
    __builtin_amdgcn_sched_barrier(0);
    stats_sub(0, 2);
    __builtin_amdgcn_sched_barrier(0);
    MMA1(4, 17, 1);
    __builtin_amdgcn_sched_barrier(0);
    stats_sub(0, 3);
    __builtin_amdgcn_sched_barrier(0);
    MMA1(5, 17, 2);
    __builtin_amdgcn_sched_barrier(0);
    stats_issue(2, 1);
    fr[0] = FRAG(a0p, 0, 1);
    __builtin_amdgcn_sched_barrier(0);
    BREAD(3, 19, 0);
    BREAD(4, 19, 1);
    BREAD(5, 19, 2);
    WAITR(17, fr[19]);
    asm volatile("" : "+v"(fr[18]));
    MMA1(0, 18, 0);
    __builtin_amdgcn_sched_barrier(0);
    stats_sub(1, 0);
    __builtin_amdgcn_sched_barrier(0);
    MMA1(1, 18, 1);
    __builtin_amdgcn_sched_barrier(0);
    stats_sub(1, 1);
    __builtin_amdgcn_sched_barrier(0);
    MMA1(2, 18, 2);
    __builtin_amdgcn_sched_barrier(0);
    fr[1] = FRAG(a1p, 0, 1);
    __builtin_amdgcn_sched_barrier(0);
    BREAD(0, 20, 0);
    BREAD(1, 20, 1);
    BREAD(2, 20, 2);
    MMA1(3, 19, 0);
    __builtin_amdgcn_sched_barrier(0);
    stats_sub(1, 2);
    __builtin_amdgcn_sched_barrier(0);
    MMA1(4, 19, 1);
    __builtin_amdgcn_sched_barrier(0);
    stats_sub(1, 3);
    __builtin_amdgcn_sched_barrier(0);
    MMA1(5, 19, 2);
    __builtin_amdgcn_sched_barrier(0);
    stats_issue(2, 2);
    fr[2] = FRAG(a0p, 1, 1);
    __builtin_amdgcn_sched_barrier(0);
    BREAD(3, 21, 0);
    BREAD(4, 21, 1);
    BREAD(5, 21, 2);
    WAITR(17, fr[21]);
    asm volatile("" : "+v"(fr[20]));
    MMA1(0, 20, 0);
    __builtin_amdgcn_sched_barrier(0);
    stats_sub(2, 0);
    __builtin_amdgcn_sched_barrier(0);
    MMA1(1, 20, 1);
    __builtin_amdgcn_sched_barrier(0);
    stats_sub(2, 1);
    __builtin_amdgcn_sched_barrier(0);
    MMA1(2, 20, 2);
    __builtin_amdgcn_sched_barrier(0);
    fr[3] = FRAG(a1p, 1, 1);
    __builtin_amdgcn_sched_barrier(0);
    BREAD(0, 22, 0);
    BREAD(1, 22, 1);
    BREAD(2, 22, 2);
    MMA1(3, 21, 0);
    __builtin_amdgcn_sched_barrier(0);
    stats_sub(2, 2);
    __builtin_amdgcn_sched_barrier(0);
    MMA1(4, 21, 1);
    __builtin_amdgcn_sched_barrier(0);
    stats_sub(2, 3);
    __builtin_amdgcn_sched_barrier(0);
    MMA1(5, 21, 2);
    __builtin_amdgcn_sched_barrier(0);
    fr[4] = FRAG(a0p, 2, 1);
    __builtin_amdgcn_sched_barrier(0);
    BREAD(3, 23, 0);
    BREAD(4, 23, 1);
    BREAD(5, 23, 2);
    WAITR(17, fr[23]);
    asm volatile("" : "+v"(fr[22]));
    MMA1(0, 22, 0);
    __builtin_amdgcn_sched_barrier(0);
    __builtin_amdgcn_sched_barrier(0);
    MMA1(1, 22, 1);
    __builtin_amdgcn_sched_barrier(0);
    __builtin_amdgcn_sched_barrier(0);
    MMA1(2, 22, 2);
    __builtin_amdgcn_sched_barrier(0);
    fr[5] = FRAG(a1p, 2, 1);
    __builtin_amdgcn_sched_barrier(0);
    WAITR(26, vo0);
    BARRIER();
    BREAD(0, 24, 0);
    BREAD(1, 24, 1);
    BREAD(2, 24, 2);
    MMA1(3, 23, 0);
    __builtin_amdgcn_sched_barrier(0);
    __builtin_amdgcn_sched_barrier(0);
    MMA1(4, 23, 1);
    __builtin_amdgcn_sched_barrier(0);
    __builtin_amdgcn_sched_barrier(0);
    MMA1(5, 23, 2);
    __builtin_amdgcn_sched_barrier(0);
    stats_issue(3, 0);
    fr[6] = FRAG(a0p, 3, 1);
    __builtin_amdgcn_sched_barrier(0);
    BREAD(3, 25, 0);
    BREAD(4, 25, 1);
    BREAD(5, 25, 2);
    WAITR(17, fr[25]);
    asm volatile("" : "+v"(fr[24]));
    MMA1(0, 24, 0);
    __builtin_amdgcn_sched_barrier(0);
    stats_sub(0, 0);
    stats_sub(0, 1);
    __builtin_amdgcn_sched_barrier(0);
    MMA1(1, 24, 1);
    __builtin_amdgcn_sched_barrier(0);
    stats_sub(0, 2);
    stats_sub(0, 3);
    __builtin_amdgcn_sched_barrier(0);
    MMA1(2, 24, 2);
    __builtin_amdgcn_sched_barrier(0);
    stats_issue(3, 1);
    fr[7] = FRAG(a1p, 3, 1);
    __builtin_amdgcn_sched_barrier(0);
    BREAD(0, 26, 0);
    BREAD(1, 26, 1);
    BREAD(2, 26, 2);
    MMA1(3, 25, 0);
    __builtin_amdgcn_sched_barrier(0);
    stats_sub(1, 0);
    stats_sub(1, 1);
    __builtin_amdgcn_sched_barrier(0);
    MMA1(4, 25, 1);
    __builtin_amdgcn_sched_barrier(0);
    stats_sub(1, 2);
    stats_sub(1, 3);
    __builtin_amdgcn_sched_barrier(0);
    MMA1(5, 25, 2);
    __builtin_amdgcn_sched_barrier(0);
    stats_issue(3, 2);
    fr[8] = FRAG(a0p, 4, 1);
    __builtin_amdgcn_sched_barrier(0);
    BREAD(3, 27, 0);
    BREAD(4, 27, 1);
    BREAD(5, 27, 2);
    WAITR(17, fr[27]);
    asm volatile("" : "+v"(fr[26]));
    MMA1(0, 26, 0);
    __builtin_amdgcn_sched_barrier(0);
    stats_sub(2, 0);
    stats_sub(2, 1);
    __builtin_amdgcn_sched_barrier(0);
    MMA1(1, 26, 1);
    __builtin_amdgcn_sched_barrier(0);
    stats_sub(2, 2);
    stats_sub(2, 3);
    __builtin_amdgcn_sched_barrier(0);
    MMA1(2, 26, 2);
    __builtin_amdgcn_sched_barrier(0);
    fr[9] = FRAG(a1p, 4, 1);
    WAITR(10, csp[0]);
#pragma unroll
    for (int k = 1; k < 4; ++k) asm volatile("" : "+v"(csp[k]));
    publish_partials();
    __builtin_amdgcn_sched_barrier(0);
    BREAD(0, 28, 0);
    BREAD(1, 28, 1);
    BREAD(2, 28, 2);
    BARRIER();
    MMA1(3, 27, 0);
    __builtin_amdgcn_sched_barrier(0);
    __builtin_amdgcn_sched_barrier(0);
    MMA1(4, 27, 1);
    __builtin_amdgcn_sched_barrier(0);
    __builtin_amdgcn_sched_barrier(0);
    MMA1(5, 27, 2);
    __builtin_amdgcn_sched_barrier(0);
    fr[10] = FRAG(a0p, 5, 1);
    finish_issue(0);
    __builtin_amdgcn_sched_barrier(0);
    BREAD(3, 29, 0);
    BREAD(4, 29, 1);
    BREAD(5, 29, 2);
    WAITR(17, fr[29]);
    asm volatile("" : "+v"(fr[28]));
    MMA1(0, 28, 0);
    __builtin_amdgcn_sched_barrier(0);
    __builtin_amdgcn_sched_barrier(0);
    MMA1(1, 28, 1);
    __builtin_amdgcn_sched_barrier(0);
    __builtin_amdgcn_sched_barrier(0);
    MMA1(2, 28, 2);
    __builtin_amdgcn_sched_barrier(0);
    fr[11] = FRAG(a1p, 5, 1);
    finish_round(0);
    finish_issue(1);
    __builtin_amdgcn_sched_barrier(0);
    BREAD(0, 30, 0);
    BREAD(1, 30, 1);
    BREAD(2, 30, 2);
    MMA1(3, 29, 0);
    __builtin_amdgcn_sched_barrier(0);
    __builtin_amdgcn_sched_barrier(0);
    MMA1(4, 29, 1);
    __builtin_amdgcn_sched_barrier(0);
    __builtin_amdgcn_sched_barrier(0);
    MMA1(5, 29, 2);
    __builtin_amdgcn_sched_barrier(0);
    fr[12] = FRAG(a0p, 6, 1);
    finish_round(1);
    __builtin_amdgcn_sched_barrier(0);
    BREAD(3, 31, 0);
    BREAD(4, 31, 1);
    BREAD(5, 31, 2);
    WAITR(17, fr[31]);
    asm volatile("" : "+v"(fr[30]));
    MMA1(0, 30, 0);
    __builtin_amdgcn_sched_barrier(0);
    __builtin_amdgcn_sched_barrier(0);
    MMA1(1, 30, 1);
    __builtin_amdgcn_sched_barrier(0);
    __builtin_amdgcn_sched_barrier(0);
    MMA1(2, 30, 2);
    __builtin_amdgcn_sched_barrier(0);
    fr[13] = FRAG(a1p, 6, 1);
    finish_rest();
    __builtin_amdgcn_sched_barrier(0);
    MMA1(3, 31, 0);
    __builtin_amdgcn_sched_barrier(0);
    __builtin_amdgcn_sched_barrier(0);
    MMA1(4, 31, 1);
    __builtin_amdgcn_sched_barrier(0);
    __builtin_amdgcn_sched_barrier(0);
    MMA1(5, 31, 2);
    __builtin_amdgcn_sched_barrier(0);
    fr[14] = FRAG(a0p, 7, 1);
    __builtin_amdgcn_sched_barrier(0);
    STAMP(1, 5);
    EPI(0);
    EPI(1);
    STAMP(1, 6);
    EPI(2);
    STAMP(1, 7);
    WAITV(0);
    __syncthreads();
    if (tid == 0) __hip_atomic_store(flags + (rb * K3_CG + cg) * 64, epoch, RLX_AGENT);
    fr[15] = FRAG(a1p, 7, 1);
    fr[16] = FRAG(a0p, 8, 1);
    fr[17] = FRAG(a1p, 8, 1);
    fr[18] = FRAG(a0p, 9, 1);
    fr[19] = FRAG(a1p, 9, 1);
    fr[20] = FRAG(a0p, 10, 1);
    fr[21] = FRAG(a1p, 10, 1);
    LOAD_RES();
  } else {
    WAIT_FLAGS(0);
    fr[0] = FRAG(a0p, 0, 1);
    fr[1] = FRAG(a1p, 0, 1);
    fr[2] = FRAG(a0p, 1, 1);
    fr[3] = FRAG(a1p, 1, 1);
    fr[4] = FRAG(a0p, 2, 1);
    fr[5] = FRAG(a1p, 2, 1);
    fr[6] = FRAG(a0p, 3, 1);
    fr[7] = FRAG(a1p, 3, 1);
    fr[8] = FRAG(a0p, 4, 1);
    fr[9] = FRAG(a1p, 4, 1);
    fr[10] = FRAG(a0p, 5, 1);
    fr[11] = FRAG(a1p, 5, 1);
    fr[12] = FRAG(a0p, 6, 1);
    fr[13] = FRAG(a1p, 6, 1);
    fr[14] = FRAG(a0p, 7, 1);
    fr[15] = FRAG(a1p, 7, 1);
    fr[16] = FRAG(a0p, 8, 1);
    fr[17] = FRAG(a1p, 8, 1);
    fr[18] = FRAG(a0p, 9, 1);
    fr[19] = FRAG(a1p, 9, 1);
    fr[20] = FRAG(a0p, 10, 1);
    fr[21] = FRAG(a1p, 10, 1);
    LOAD_RES();
  }
  STAMP(1, 8);

  {
    const char* b0p = ws + WS_HM + ((size_t)(2 * rb2) * NFT + ks0) * 1024;
    const char* b1p = b0p + (size_t)NFT * 1024;
    u32x4 B0[11], B1[11];
    if (wave == 0) {
      const int lA = 64 * rb2, lB = lA + 63;
      const int rA = min(lA / 96, K3_RB - 1), rB = min(lB / 96, K3_RB - 1);
      const int nfl = (rB - rA + 1) * K3_CG;
      const int fi = rA * K3_CG + min(lane, nfl - 1);
      for (unsigned spins = 0;; ++spins) {
        const unsigned v = __hip_atomic_load(flags + fi * 64, RLX_AGENT);
        if (__all(v == epoch) || spins > SPIN_CAP) break;
        __builtin_amdgcn_s_sleep(1);
      }
    }
    __syncthreads();
    STAMP(1, 9);
#define MMA2(k)                                                                                                   \
  do {                                                                                                            \
    const bf16x8 a0 = __builtin_bit_cast(bf16x8, fr[2 * (k)]), a1 = __builtin_bit_cast(bf16x8, fr[2 * (k) + 1]);  \
    const bf16x8 b0 = __builtin_bit_cast(bf16x8, B0[k]), b1 = __builtin_bit_cast(bf16x8, B1[k]);                  \
    acc[0][0] = __builtin_amdgcn_mfma_f32_32x32x16_bf16(a0, b0, acc[0][0], 0, 0, 0);                              \
    acc[0][1] = __builtin_amdgcn_mfma_f32_32x32x16_bf16(a0, b1, acc[0][1], 0, 0, 0);                              \
    acc[1][0] = __builtin_amdgcn_mfma_f32_32x32x16_bf16(a1, b0, acc[1][0], 0, 0, 0);                              \
    acc[1][1] = __builtin_amdgcn_mfma_f32_32x32x16_bf16(a1, b1, acc[1][1], 0, 0, 0);                              \
  } while (0)
    B0[0] = FRAG(b0p, 0, 1);
    B1[0] = FRAG(b1p, 0, 1);
    B0[1] = FRAG(b0p, 1, 1);
    B1[1] = FRAG(b1p, 1, 1);
    B0[2] = FRAG(b0p, 2, 1);
    B1[2] = FRAG(b1p, 2, 1);
    B0[3] = FRAG(b0p, 3, 1);
    B1[3] = FRAG(b1p, 3, 1);
    B0[4] = FRAG(b0p, 4, 1);
    B1[4] = FRAG(b1p, 4, 1);
    B0[5] = FRAG(b0p, 5, 1);
    B1[5] = FRAG(b1p, 5, 1);
    B0[6] = FRAG(b0p, 6, 1);
    B1[6] = FRAG(b1p, 6, 1);
    B0[7] = FRAG(b0p, 7, 1);
    B1[7] = FRAG(b1p, 7, 1);
    B0[8] = FRAG(b0p, 8, 1);
    B1[8] = FRAG(b1p, 8, 1);
    B0[9] = FRAG(b0p, 9, 1);
    B1[9] = FRAG(b1p, 9, 1);
    B0[10] = FRAG(b0p, 10, 1);
    B1[10] = FRAG(b1p, 10, 1);
    f32x16 acc[2][2] = {};
    asm volatile("s_waitcnt vmcnt(20)" : "+v"(fr[0]), "+v"(fr[1]), "+v"(B0[0]), "+v"(B1[0]));
    MMA2(0);
    asm volatile("s_waitcnt vmcnt(18)" : "+v"(fr[2]), "+v"(fr[3]), "+v"(B0[1]), "+v"(B1[1]));
    MMA2(1);
    asm volatile("s_waitcnt vmcnt(16)" : "+v"(fr[4]), "+v"(fr[5]), "+v"(B0[2]), "+v"(B1[2]));
    MMA2(2);
    asm volatile("s_waitcnt vmcnt(14)" : "+v"(fr[6]), "+v"(fr[7]), "+v"(B0[3]), "+v"(B1[3]));
    MMA2(3);
    asm volatile("s_waitcnt vmcnt(12)" : "+v"(fr[8]), "+v"(fr[9]), "+v"(B0[4]), "+v"(B1[4]));
    MMA2(4);
    asm volatile("s_waitcnt vmcnt(10)" : "+v"(fr[10]), "+v"(fr[11]), "+v"(B0[5]), "+v"(B1[5]));
    MMA2(5);
    asm volatile("s_waitcnt vmcnt(8)" : "+v"(fr[12]), "+v"(fr[13]), "+v"(B0[6]), "+v"(B1[6]));
    MMA2(6);
    asm volatile("s_waitcnt vmcnt(6)" : "+v"(fr[14]), "+v"(fr[15]), "+v"(B0[7]), "+v"(B1[7]));
    MMA2(7);
    asm volatile("s_waitcnt vmcnt(4)" : "+v"(fr[16]), "+v"(fr[17]), "+v"(B0[8]), "+v"(B1[8]));
    MMA2(8);
    asm volatile("s_waitcnt vmcnt(2)" : "+v"(fr[18]), "+v"(fr[19]), "+v"(B0[9]), "+v"(B1[9]));
    MMA2(9);
    asm volatile("s_waitcnt vmcnt(0)" : "+v"(fr[20]), "+v"(fr[21]), "+v"(B0[10]), "+v"(B1[10]));
    if (k11) MMA2(10);
    STAMP(1, 10);
#pragma unroll
    for (int t = 0; t < 4; ++t) {
#pragma unroll
      for (int hf = 0; hf < 2; ++hf) {
        const int ow = 2 * t + hf;
        if (ow != wave) {
          const int sl = (wave - ow - 1) & 7;
          char* dst = smem + (ow * 7 + sl) * 2048 + lane * 16;
          const f32x16& a = acc[t >> 1][t & 1];
          *(f32x4*)(dst) = (f32x4){a[8 * hf], a[8 * hf + 1], a[8 * hf + 2], a[8 * hf + 3]};
          *(f32x4*)(dst + 1024) = (f32x4){a[8 * hf + 4], a[8 * hf + 5], a[8 * hf + 6], a[8 * hf + 7]};
        }
      }
    }
    __syncthreads();
    float fin[8];
#pragma unroll
    for (int t = 0; t < 4; ++t)
#pragma unroll
      for (int hf = 0; hf < 2; ++hf)
        if (2 * t + hf == wave) {
#pragma unroll
          for (int k = 0; k < 8; ++k) fin[k] = acc[t >> 1][t & 1][8 * hf + k];
        }
#pragma unroll
    for (int s = 0; s < 7; ++s) {
      const char* src = smem + (wave * 7 + s) * 2048 + lane * 16;
      const f32x4 v0 = *(const f32x4*)src, v1 = *(const f32x4*)(src + 1024);
      fin[0] += v0[0];
      fin[1] += v0[1];
      fin[2] += v0[2];
      fin[3] += v0[3];
      fin[4] += v1[0];
      fin[5] += v1[1];
      fin[6] += v1[2];
      fin[7] += v1[3];
    }
#pragma unroll
    for (int k = 0; k < 8; ++k) asm volatile("" : "+v"(res[k]));
    u16* op = out + (size_t)lpos * D_ + dbase + 4 * h;
#pragma unroll
    for (int i = 0; i < 2; ++i) {
      u32x2 o;
      o[0] = pack_bf2(bflo(res[4 * i]) + rbf(fin[4 * i]), bflo(res[4 * i + 1]) + rbf(fin[4 * i + 1]));
      o[1] = pack_bf2(bflo(res[4 * i + 2]) + rbf(fin[4 * i + 2]), bflo(res[4 * i + 3]) + rbf(fin[4 * i + 3]));
      *(u32x2*)(op + 8 * i) = o;
    }
  }
  STAMP(1, 15);
  STAMP_FLUSH(1);
  }
}

}

extern "C" void kernel_launch(void* const* d_in, const int* in_sizes, int n_in, void* d_out, int out_size, void* d_ws,
                              size_t ws_size, hipStream_t stream) {
  const u16* x = (const u16*)d_in[0];
  const u16* lar = (const u16*)d_in[1];
  const u16* aim = (const u16*)d_in[2];
  const u16* cre = (const u16*)d_in[3];
  const u16* cim = (const u16*)d_in[4];
  const u16* logdt = (const u16*)d_in[5];
  const u16* dskip = (const u16*)d_in[6];
  const u16* Wg = (const u16*)d_in[8];
  const u16* Wu = (const u16*)d_in[9];
  const u16* Wd = (const u16*)d_in[10];
  char* ws = (char*)d_ws;
  constexpr int LDS_BYTES = K12_LDS > K34_LDS ? K12_LDS : K34_LDS;
  (void)hipFuncSetAttribute((const void*)kall, hipFuncAttributeMaxDynamicSharedMemorySize, LDS_BYTES);
  kall<<<256, 512, LDS_BYTES, stream>>>(x, lar, aim, cre, cim, logdt, dskip, Wg, Wu, Wd, ws, (u16*)d_out);
}
```
